```python
import torch

def _softmax(x, axis=-1):
    return torch.softmax(x, dim=axis)
def _gelu(x):
    return torch.nn.functional.gelu(x, approximate="tanh")
def _silu(x):
    return torch.nn.functional.silu(x)
def _sigmoid(x):
    return torch.sigmoid(x)
def _layernorm(x, axis=-1, eps=1e-5):
    m = x.mean(dim=axis, keepdim=True)
    v = x.var(dim=axis, keepdim=True, unbiased=False)
    return (x - m) / torch.sqrt(v + eps)
def _scatter_add(tgt, idx, src, axis=0):
    return tgt.clone().index_add_(axis, idx, src)
def _segment_sum(src, seg, n):
    out = torch.zeros((n,) + src.shape[1:], dtype=src.dtype)
    return out.index_add_(0, seg, src)
def _pad(x, pw):
    flat = tuple(v for pair in reversed(pw) for v in pair)
    return torch.nn.functional.pad(x, flat)

kernel_name = 'sequential_preLN__linear_attn_block__glu_gated_bilinear__prompt'

def setup_inputs():

    _w = (lambda a: (lambda t: t.to(torch.bfloat16) if t.is_floating_point() else t)(torch.as_tensor(a)))
    def _A_setup():
        B, S, D = 1, 2048, 448
        return {
            "x": _w((lambda *s: torch.randn(*s, dtype=torch.bfloat16) * 0.05)(B, S, D)),
            "wqkv": _w((lambda *s: torch.randn(*s, dtype=torch.bfloat16) * 0.1)(D, 3 * D)),
            "wo": _w((lambda *s: torch.randn(*s, dtype=torch.bfloat16) * 0.05)(D, D)),
        }
    _A_in = _A_setup()

    def _B_setup():
        B, S, D, F = 1, 2048, 448, 1792
        return {
            "x": _w((lambda *s: torch.randn(*s, dtype=torch.bfloat16) * 0.05)(B, S, D)),
            "w_gate": _w((lambda *s: torch.randn(*s, dtype=torch.bfloat16) * 0.4)(D, F) * (D ** -0.5)),
            "w_up": _w((lambda *s: torch.randn(*s, dtype=torch.bfloat16) * 0.4)(D, F) * (D ** -0.5)),
            "w_down": _w((lambda *s: torch.randn(*s, dtype=torch.bfloat16) * 0.4)(F, D) * (F ** -0.5)),
        }
    _B_in = _B_setup()

    return {
        "A_x": _A_in["x"],
        "A_wqkv": _A_in["wqkv"],
        "A_wo": _A_in["wo"],
        "B_x": _B_in["x"],
        "B_w_gate": _B_in["w_gate"],
        "B_w_up": _B_in["w_up"],
        "B_w_down": _B_in["w_down"],
    }

def reference(A_x, A_wqkv, A_wo, B_x, B_w_gate, B_w_up, B_w_down):
    def _A_ref(x, wqkv, wo):
        B, S, D = x.shape
        H = 16; Dh = D // H
        qkv = (lambda x, *d: x.permute(*d))((x @ wqkv).reshape(B, S, 3, H, Dh), 2, 0, 3, 1, 4)
        q, k, v = qkv[0], qkv[1], qkv[2]
        phi = lambda t: torch.where(t > 0, t + 1, torch.exp(t)) + 1e-6
        pq, pk = phi(q), phi(k)
        kv = torch.einsum("bhsd,bhse->bhde", pk, v)
        z = torch.einsum("bhsd->bhd", pk)
        num = torch.einsum("bhqd,bhde->bhqe", pq, kv)
        den = torch.einsum("bhqd,bhd->bhq", pq, z)[..., None] + 1e-6
        o = num / den
        return (lambda x, *d: x.permute(*d))(o, 0, 2, 1, 3).reshape(B, S, D) @ wo

    def _B_ref(x, w_gate, w_up, w_down):
        g = torch.einsum("bsd,df->bsf", x, w_gate)
        u = torch.einsum("bsd,df->bsf", x, w_up)
        h = g * u
        return torch.einsum("bsf,fd->bsd", h, w_down)

    x = A_x
    h = x + _A_ref(_layernorm(x, axis=-1), A_wqkv, A_wo)
    h2 = h + _B_ref(_layernorm(h, axis=-1), B_w_gate, B_w_up, B_w_down)
    return h2
```

```cpp
#include <hip/hip_runtime.h>
#include <hip/hip_bf16.h>
#include <stdint.h>

typedef __attribute__((ext_vector_type(8))) __bf16 bf16x8;
typedef __attribute__((ext_vector_type(4))) __bf16 bf16x4;
typedef __attribute__((ext_vector_type(2))) __bf16 bf16x2;
typedef __attribute__((ext_vector_type(4))) short s16x4;
typedef __attribute__((ext_vector_type(8))) short s16x8;
typedef __attribute__((ext_vector_type(16))) float f32x16;
typedef __attribute__((ext_vector_type(4))) float f32x4;
typedef __attribute__((ext_vector_type(4))) unsigned u32x4;
typedef __attribute__((ext_vector_type(2))) unsigned u32x2;
typedef unsigned short u16;
#define DI __device__ __forceinline__
#define S_ 2048
#define D_ 448
#define F_ 1792

DI f32x16 mfma(bf16x8 a, bf16x8 b, f32x16 c) { return __builtin_amdgcn_mfma_f32_32x32x16_bf16(a, b, c, 0, 0, 0); }
DI s16x4 trr(const char* p) { return __builtin_amdgcn_ds_read_tr16_b64_v4i16((__attribute__((address_space(3))) s16x4*)p); }
DI bf16x8 bfrag(const char* img, int ks, int lane) {
  const int hf = lane >> 5, blk = (lane >> 4) & 1, q = (lane & 15) >> 2, p = lane & 3;
  const char* a = img + (16 * ks + 8 * hf + q) * 64 + blk * 32 + p * 8;
  s16x4 lo = trr(a), hi = trr(a + 256);
  s16x8 r = __builtin_shufflevector(lo, hi, 0, 1, 2, 3, 4, 5, 6, 7);
  return __builtin_bit_cast(bf16x8, r);
}
DI float bf2f(__bf16 v) { return (float)v; }
DI float phi(float t) { t = (float)(__bf16)t; float r = t > 0.f ? t + 1.f : __expf(t); return r + 1e-6f; }

template <int NKS> DI void layernorm_frags(bf16x8 (&a)[NKS]) {
  float s = 0.f;
#pragma unroll
  for (int k = 0; k < NKS; ++k)
#pragma unroll
    for (int j = 0; j < 8; ++j) s += bf2f(a[k][j]);
  s += __shfl_xor(s, 32);
  const float m = s * (1.f / D_);
  float v = 0.f;
#pragma unroll
  for (int k = 0; k < NKS; ++k)
#pragma unroll
    for (int j = 0; j < 8; ++j) { float d = bf2f(a[k][j]) - m; v += d * d; }
  v += __shfl_xor(v, 32);
  const float rs = rsqrtf(v * (1.f / D_) + 1e-5f);
#pragma unroll
  for (int k = 0; k < NKS; ++k)
#pragma unroll
    for (int j = 0; j < 8; ++j) a[k][j] = (__bf16)((bf2f(a[k][j]) - m) * rs);
}

__global__ __launch_bounds__(256, 1) void k1(const u16* __restrict__ x, const u16* __restrict__ wqkv, u16* __restrict__ pq, float* __restrict__ part) {
  __shared__ __attribute__((aligned(16))) char lds[3 * 448 * 64];
  const int b = blockIdx.x, xcd = b & 7, i = b >> 3, h = 2 * xcd + (i >> 4), p = i & 15;
  const int tid = threadIdx.x, lane = tid & 63, w = tid >> 6, r = lane & 31, hf = lane >> 5;
  bf16x8 a[28];
  const u16* xr = x + (size_t)(p * 128 + w * 32 + r) * D_ + 8 * hf;
#pragma unroll
  for (int k = 0; k < 28; ++k) a[k] = *(const bf16x8*)(xr + 16 * k);
  for (int n = tid; n < 3 * 448 * 8; n += 256) {
    const int c = n & 7, k = (n >> 3) % 448, t = (n >> 3) / 448;
    u32x2 v = {0u, 0u};
    if (c < 7) v = *(const u32x2*)(wqkv + (size_t)k * 1344 + t * 448 + 28 * h + 4 * c);
    *(u32x2*)(lds + (t * 448 + k) * 64 + c * 8) = v;
  }
  layernorm_frags<28>(a);
  __syncthreads();
  f32x16 cq = {0}, ck = {0}, cv = {0};
#pragma unroll
  for (int k = 0; k < 28; ++k) {
    cq = mfma(a[k], bfrag(lds, k, lane), cq);
    ck = mfma(a[k], bfrag(lds + 448 * 64, k, lane), ck);
    cv = mfma(a[k], bfrag(lds + 2 * 448 * 64, k, lane), cv);
  }
  bf16x8 pk[2], vv[2];
#pragma unroll
  for (int j = 0; j < 16; ++j) {
    pk[j >> 3][j & 7] = (__bf16)phi(ck[j]);
    vv[j >> 3][j & 7] = (r == 28) ? (__bf16)1.0f : (__bf16)cv[j];
    const int row = p * 128 + w * 32 + (j & 3) + 8 * (j >> 2) + 4 * hf;
    if (r < 28) pq[(size_t)row * D_ + 28 * h + r] = __builtin_bit_cast(u16, (__bf16)phi(cq[j]));
  }
  f32x16 kv = {0};
  kv = mfma(pk[0], vv[0], kv);
  kv = mfma(pk[1], vv[1], kv);
  __syncthreads();
  float* red = (float*)lds;
#pragma unroll
  for (int j = 0; j < 16; ++j) red[w * 1024 + j * 64 + lane] = kv[j];
  __syncthreads();
  f32x4 s = *(f32x4*)(red + tid * 4) + *(f32x4*)(red + 1024 + tid * 4) + *(f32x4*)(red + 2048 + tid * 4) + *(f32x4*)(red + 3072 + tid * 4);
  *(f32x4*)(part + (size_t)(h * 16 + p) * 1024 + tid * 4) = s;
}

__global__ __launch_bounds__(256, 1) void k2(const u16* __restrict__ pq, const float* __restrict__ part, u16* __restrict__ o) {
  __shared__ __attribute__((aligned(16))) char lds[32 * 64 + 64];
  const int b = blockIdx.x, xcd = b & 7, i = b >> 3, h = 2 * xcd + (i >> 4), p = i & 15;
  const int tid = threadIdx.x, lane = tid & 63, w = tid >> 6, r = lane & 31, hf = lane >> 5;
  f32x4 s = {0, 0, 0, 0};
#pragma unroll
  for (int q = 0; q < 16; ++q) s += *(const f32x4*)(part + (size_t)(h * 16 + q) * 1024 + tid * 4);
  {
    const int j = tid >> 4, l0 = (tid & 15) * 4, d = (j & 3) + 8 * (j >> 2) + 4 * (l0 >> 5), e = l0 & 31;
    bf16x4 t;
#pragma unroll
    for (int u = 0; u < 4; ++u) t[u] = d < 28 ? (__bf16)s[u] : (__bf16)0.f;
    *(bf16x4*)(lds + d * 64 + e * 2) = t;
  }
  const u16* pr = pq + (size_t)(p * 128 + w * 32 + r) * D_ + 28 * h;
  bf16x8 a[2];
  {
    u32x2 t0 = *(const u32x2*)(pr + 8 * hf), t1 = *(const u32x2*)(pr + 8 * hf + 4), t2 = *(const u32x2*)(pr + 16 + 8 * hf), t3 = {0u, 0u};
    if (hf == 0) t3 = *(const u32x2*)(pr + 20);
    u32x4 f0 = {t0[0], t0[1], t1[0], t1[1]}, f1 = {t2[0], t2[1], t3[0], t3[1]};
    a[0] = __builtin_bit_cast(bf16x8, f0); a[1] = __builtin_bit_cast(bf16x8, f1);
  }
  __syncthreads();
  f32x16 c = {0};
  c = mfma(a[0], bfrag(lds, 0, lane), c);
  c = mfma(a[1], bfrag(lds, 1, lane), c);
#pragma unroll
  for (int j = 0; j < 16; ++j) {
    const float den = __shfl(c[j], 28 + 32 * hf) + 1e-6f;
    const int row = p * 128 + w * 32 + (j & 3) + 8 * (j >> 2) + 4 * hf;
    if (r < 28) o[(size_t)row * D_ + 28 * h + r] = __builtin_bit_cast(u16, (__bf16)(c[j] / den));
  }
}

__global__ __launch_bounds__(256, 1) void k3(const u16* __restrict__ o, const u16* __restrict__ wo, const u16* __restrict__ x, u16* __restrict__ hb) {
  __shared__ __attribute__((aligned(16))) char lds[2 * 448 * 64];
  const int b = blockIdx.x, xcd = b & 7, j7 = b >> 3, rg = 4 * xcd + j7 / 7, cg = j7 % 7;
  const int tid = threadIdx.x, lane = tid & 63, w = tid >> 6, r = lane & 31, hf = lane >> 5, st = w & 1, ct = w >> 1;
  bf16x8 a[28];
  const u16* orow = o + (size_t)(rg * 64 + st * 32 + r) * D_ + 8 * hf;
#pragma unroll
  for (int k = 0; k < 28; ++k) a[k] = *(const bf16x8*)(orow + 16 * k);
  for (int n = tid; n < 448 * 8; n += 256) {
    const int c = n & 7, k = n >> 3;
    *(u32x4*)(lds + ((c >> 2) * 448 + k) * 64 + (c & 3) * 16) = *(const u32x4*)(wo + (size_t)k * D_ + cg * 64 + c * 8);
  }
  __syncthreads();
  f32x16 c = {0};
#pragma unroll
  for (int k = 0; k < 28; ++k) c = mfma(a[k], bfrag(lds + ct * 448 * 64, k, lane), c);
#pragma unroll
  for (int j = 0; j < 16; ++j) {
    const size_t idx = (size_t)(rg * 64 + st * 32 + (j & 3) + 8 * (j >> 2) + 4 * hf) * D_ + cg * 64 + ct * 32 + r;
    hb[idx] = __builtin_bit_cast(u16, (__bf16)(bf2f(__builtin_bit_cast(__bf16, x[idx])) + (float)(__bf16)c[j]));
  }
}

__global__ __launch_bounds__(256, 1) void k4(const u16* __restrict__ hb, const u16* __restrict__ wg, const u16* __restrict__ wu, u16* __restrict__ hh) {
  __shared__ __attribute__((aligned(16))) char lds[4 * 448 * 64];
  const int b = blockIdx.x, RG = b & 7, fc = b >> 3;
  const int tid = threadIdx.x, lane = tid & 63, w = tid >> 6, r = lane & 31, hf = lane >> 5;
  for (int n = tid; n < 2 * 448 * 8; n += 256) {
    const int c = n & 7, k = (n >> 3) % 448, t = (n >> 3) / 448;
    *(u32x4*)(lds + ((t * 2 + (c >> 2)) * 448 + k) * 64 + (c & 3) * 16) = *(const u32x4*)((t ? wu : wg) + (size_t)k * F_ + fc * 64 + c * 8);
  }
  __syncthreads();
#pragma unroll 1
  for (int half = 0; half < 2; ++half) {
    bf16x8 a[28];
    const int row0 = RG * 256 + w * 64 + half * 32;
    const u16* hr = hb + (size_t)(row0 + r) * D_ + 8 * hf;
#pragma unroll
    for (int k = 0; k < 28; ++k) a[k] = *(const bf16x8*)(hr + 16 * k);
    layernorm_frags<28>(a);
    f32x16 g0 = {0}, g1 = {0}, u0 = {0}, u1 = {0};
#pragma unroll
    for (int k = 0; k < 28; ++k) {
      g0 = mfma(bfrag(lds, k, lane), a[k], g0);
      g1 = mfma(bfrag(lds + 448 * 64, k, lane), a[k], g1);
      u0 = mfma(bfrag(lds + 2 * 448 * 64, k, lane), a[k], u0);
      u1 = mfma(bfrag(lds + 3 * 448 * 64, k, lane), a[k], u1);
    }
    u16* dst = hh + (size_t)(row0 + r) * F_ + fc * 64 + 4 * hf;
#pragma unroll
    for (int q = 0; q < 4; ++q) {
      bf16x4 t0, t1;
#pragma unroll
      for (int u = 0; u < 4; ++u) {
        t0[u] = (__bf16)((float)(__bf16)g0[4 * q + u] * (float)(__bf16)u0[4 * q + u]);
        t1[u] = (__bf16)((float)(__bf16)g1[4 * q + u] * (float)(__bf16)u1[4 * q + u]);
      }
      *(bf16x4*)(dst + 8 * q) = t0;
      *(bf16x4*)(dst + 32 + 8 * q) = t1;
    }
  }
}

__global__ __launch_bounds__(256, 1) void k5(const u16* __restrict__ hh, const u16* __restrict__ wd, const u16* __restrict__ hb, u16* __restrict__ out) {
  __shared__ __attribute__((aligned(16))) char lds[4 * 2 * 224 * 64];
  const int b = blockIdx.x, xcd = b & 7, j7 = b >> 3, rg = 4 * xcd + j7 / 7, cg = j7 % 7;
  const int tid = threadIdx.x, lane = tid & 63, w = tid >> 6, r = lane & 31, hf = lane >> 5;
  f32x16 c00 = {0}, c01 = {0}, c10 = {0}, c11 = {0};
  char* my = lds + w * 2 * 224 * 64;
#pragma unroll 1
  for (int half = 0; half < 2; ++half) {
    const int k0 = w * 448 + half * 224;
    bf16x8 a0[14], a1[14];
    const u16* hr = hh + (size_t)(rg * 64 + r) * F_ + k0 + 8 * hf;
#pragma unroll
    for (int k = 0; k < 14; ++k) { a0[k] = *(const bf16x8*)(hr + 16 * k); a1[k] = *(const bf16x8*)(hr + 32 * F_ + 16 * k); }
    if (half) __syncthreads();
    for (int n = lane; n < 224 * 8; n += 64) {
      const int c = n & 7, k = n >> 3;
      *(u32x4*)(my + ((c >> 2) * 224 + k) * 64 + (c & 3) * 16) = *(const u32x4*)(wd + (size_t)(k0 + k) * D_ + cg * 64 + c * 8);
    }
    __syncthreads();
#pragma unroll
    for (int k = 0; k < 14; ++k) {
      bf16x8 b0 = bfrag(my, k, lane), b1 = bfrag(my + 224 * 64, k, lane);
      c00 = mfma(a0[k], b0, c00); c01 = mfma(a0[k], b1, c01);
      c10 = mfma(a1[k], b0, c10); c11 = mfma(a1[k], b1, c11);
    }
  }
  __syncthreads();
  float* red = (float*)lds;
#pragma unroll
  for (int j = 0; j < 16; ++j) {
    red[(w * 4 + 0) * 1024 + j * 64 + lane] = c00[j];
    red[(w * 4 + 1) * 1024 + j * 64 + lane] = c01[j];
    red[(w * 4 + 2) * 1024 + j * 64 + lane] = c10[j];
    red[(w * 4 + 3) * 1024 + j * 64 + lane] = c11[j];
  }
  __syncthreads();
  const int st = w >> 1, ct = w & 1;
#pragma unroll
  for (int j = 0; j < 16; ++j) {
    const float s = red[(0 + w) * 1024 + j * 64 + lane] + red[(4 + w) * 1024 + j * 64 + lane] + red[(8 + w) * 1024 + j * 64 + lane] + red[(12 + w) * 1024 + j * 64 + lane];
    const size_t idx = (size_t)(rg * 64 + st * 32 + (j & 3) + 8 * (j >> 2) + 4 * hf) * D_ + cg * 64 + ct * 32 + r;
    out[idx] = __builtin_bit_cast(u16, (__bf16)(bf2f(__builtin_bit_cast(__bf16, hb[idx])) + (float)(__bf16)s));
  }
}

extern "C" void kernel_launch(void* const* d_in, const int* in_sizes, int n_in, void* d_out, int out_size, void* d_ws, size_t ws_size, hipStream_t stream) {
  const u16* x = (const u16*)d_in[0]; const u16* wqkv = (const u16*)d_in[1]; const u16* wo = (const u16*)d_in[2];
  const u16* wg = (const u16*)d_in[4]; const u16* wu = (const u16*)d_in[5]; const u16* wd = (const u16*)d_in[6];
  char* ws = (char*)d_ws;
  u16* pq = (u16*)(ws + 0x000000); float* part = (float*)(ws + 0x200000); u16* o = (u16*)(ws + 0x400000); u16* hb = (u16*)(ws + 0x600000); u16* hh = (u16*)(ws + 0x800000);
  k1<<<256, 256, 0, stream>>>(x, wqkv, pq, part);
  k2<<<256, 256, 0, stream>>>(pq, part, o);
  k3<<<224, 256, 0, stream>>>(o, wo, x, hb);
  k4<<<224, 256, 0, stream>>>(hb, wg, wu, hh);
  k5<<<224, 256, 0, stream>>>(hh, wd, hb, (u16*)d_out);
}
```

```cpp
#include <hip/hip_runtime.h>
#include <hip/hip_bf16.h>
#include <stdint.h>

typedef __attribute__((ext_vector_type(8))) __bf16 bf16x8;
typedef __attribute__((ext_vector_type(4))) __bf16 bf16x4;
typedef __attribute__((ext_vector_type(2))) __bf16 bf16x2;
typedef __attribute__((ext_vector_type(4))) short s16x4;
typedef __attribute__((ext_vector_type(8))) short s16x8;
typedef __attribute__((ext_vector_type(16))) float f32x16;
typedef __attribute__((ext_vector_type(4))) float f32x4;
typedef __attribute__((ext_vector_type(4))) unsigned u32x4;
typedef __attribute__((ext_vector_type(2))) unsigned u32x2;
typedef unsigned short u16;
#define DI __device__ __forceinline__
#define S_ 2048
#define D_ 448
#define F_ 1792

DI f32x16 mfma(bf16x8 a, bf16x8 b, f32x16 c) { return __builtin_amdgcn_mfma_f32_32x32x16_bf16(a, b, c, 0, 0, 0); }
DI s16x4 trr(const char* p) { return __builtin_amdgcn_ds_read_tr16_b64_v4i16((__attribute__((address_space(3))) s16x4*)p); }
DI bf16x8 bfrag(const char* img, int ks, int lane) {
  const int hf = lane >> 5, blk = (lane >> 4) & 1, q = (lane & 15) >> 2, p = lane & 3;
  const char* a = img + (16 * ks + 8 * hf + q) * 64 + blk * 32 + p * 8;
  s16x4 lo = trr(a), hi = trr(a + 256);
  s16x8 r = __builtin_shufflevector(lo, hi, 0, 1, 2, 3, 4, 5, 6, 7);
  return __builtin_bit_cast(bf16x8, r);
}
DI float bf2f(__bf16 v) { return (float)v; }
DI float phi(float t) { t = (float)(__bf16)t; float r = t > 0.f ? t + 1.f : __expf(t); return r + 1e-6f; }

template <int NKS> DI void layernorm_frags(bf16x8 (&a)[NKS]) {
  float s = 0.f;
#pragma unroll
  for (int k = 0; k < NKS; ++k)
#pragma unroll
    for (int j = 0; j < 8; ++j) s += bf2f(a[k][j]);
  s += __shfl_xor(s, 32);
  const float m = s * (1.f / D_);
  float v = 0.f;
#pragma unroll
  for (int k = 0; k < NKS; ++k)
#pragma unroll
    for (int j = 0; j < 8; ++j) { float d = bf2f(a[k][j]) - m; v += d * d; }
  v += __shfl_xor(v, 32);
  const float rs = rsqrtf(v * (1.f / D_) + 1e-5f);
#pragma unroll
  for (int k = 0; k < NKS; ++k)
#pragma unroll
    for (int j = 0; j < 8; ++j) a[k][j] = (__bf16)((bf2f(a[k][j]) - m) * rs);
}

__global__ __launch_bounds__(256, 1) void k1(const u16* __restrict__ x, const u16* __restrict__ wqkv, u16* __restrict__ pq, float* __restrict__ part) {
  __shared__ __attribute__((aligned(16))) char lds[3 * 448 * 64];
  const int b = blockIdx.x, xcd = b & 7, i = b >> 3, h = 2 * xcd + (i >> 4), p = i & 15;
  const int tid = threadIdx.x, lane = tid & 63, w = tid >> 6, r = lane & 31, hf = lane >> 5;
  bf16x8 a[28];
  const u16* xr = x + (size_t)(p * 128 + w * 32 + r) * D_ + 8 * hf;
#pragma unroll
  for (int k = 0; k < 28; ++k) a[k] = *(const bf16x8*)(xr + 16 * k);
  {
    const int c = tid & 7, k0 = tid >> 3;
#pragma unroll
    for (int t = 0; t < 3; ++t) {
      u32x2 v[14];
#pragma unroll
      for (int n = 0; n < 14; ++n) v[n] = *(const u32x2*)(wqkv + (size_t)(k0 + 32 * n) * 1344 + t * 448 + 28 * h + 4 * (c < 7 ? c : 6));
#pragma unroll
      for (int n = 0; n < 14; ++n) *(u32x2*)(lds + (t * 448 + k0 + 32 * n) * 64 + c * 8) = v[n];
    }
  }
  layernorm_frags<28>(a);
  __syncthreads();
  f32x16 cq = {0}, ck = {0}, cv = {0};
#pragma unroll
  for (int k = 0; k < 28; ++k) {
    cq = mfma(a[k], bfrag(lds, k, lane), cq);
    ck = mfma(a[k], bfrag(lds + 448 * 64, k, lane), ck);
    cv = mfma(a[k], bfrag(lds + 2 * 448 * 64, k, lane), cv);
  }
  bf16x8 pk[2], vv[2];
#pragma unroll
  for (int j = 0; j < 16; ++j) {
    pk[j >> 3][j & 7] = (__bf16)phi(ck[j]);
    vv[j >> 3][j & 7] = (r == 28) ? (__bf16)1.0f : (__bf16)cv[j];
    const int row = p * 128 + w * 32 + (j & 3) + 8 * (j >> 2) + 4 * hf;
    if (r < 28) pq[(size_t)row * D_ + 28 * h + r] = __builtin_bit_cast(u16, (__bf16)phi(cq[j]));
  }
  f32x16 kv = {0};
  kv = mfma(pk[0], vv[0], kv);
  kv = mfma(pk[1], vv[1], kv);
  __syncthreads();
  float* red = (float*)lds;
#pragma unroll
  for (int j = 0; j < 16; ++j) red[w * 1024 + j * 64 + lane] = kv[j];
  __syncthreads();
  f32x4 s = *(f32x4*)(red + tid * 4) + *(f32x4*)(red + 1024 + tid * 4) + *(f32x4*)(red + 2048 + tid * 4) + *(f32x4*)(red + 3072 + tid * 4);
  *(f32x4*)(part + (size_t)(h * 16 + p) * 1024 + tid * 4) = s;
}

__global__ __launch_bounds__(256, 1) void k2(const u16* __restrict__ pq, const float* __restrict__ part, u16* __restrict__ o) {
  __shared__ __attribute__((aligned(16))) char lds[32 * 64 + 64];
  const int b = blockIdx.x, xcd = b & 7, i = b >> 3, h = 2 * xcd + (i >> 4), p = i & 15;
  const int tid = threadIdx.x, lane = tid & 63, w = tid >> 6, r = lane & 31, hf = lane >> 5;
  f32x4 s = {0, 0, 0, 0};
#pragma unroll
  for (int q = 0; q < 16; ++q) s += *(const f32x4*)(part + (size_t)(h * 16 + q) * 1024 + tid * 4);
  {
    const int j = tid >> 4, l0 = (tid & 15) * 4, d = (j & 3) + 8 * (j >> 2) + 4 * (l0 >> 5), e = l0 & 31;
    bf16x4 t;
#pragma unroll
    for (int u = 0; u < 4; ++u) t[u] = d < 28 ? (__bf16)s[u] : (__bf16)0.f;
    *(bf16x4*)(lds + d * 64 + e * 2) = t;
  }
  const u16* pr = pq + (size_t)(p * 128 + w * 32 + r) * D_ + 28 * h;
  bf16x8 a[2];
  {
    u32x2 t0 = *(const u32x2*)(pr + 8 * hf), t1 = *(const u32x2*)(pr + 8 * hf + 4), t2 = *(const u32x2*)(pr + 16 + 8 * hf), t3 = {0u, 0u};
    if (hf == 0) t3 = *(const u32x2*)(pr + 20);
    u32x4 f0 = {t0[0], t0[1], t1[0], t1[1]}, f1 = {t2[0], t2[1], t3[0], t3[1]};
    a[0] = __builtin_bit_cast(bf16x8, f0); a[1] = __builtin_bit_cast(bf16x8, f1);
  }
  __syncthreads();
  f32x16 c = {0};
  c = mfma(a[0], bfrag(lds, 0, lane), c);
  c = mfma(a[1], bfrag(lds, 1, lane), c);
#pragma unroll
  for (int j = 0; j < 16; ++j) {
    const float den = __shfl(c[j], 28 + 32 * hf) + 1e-6f;
    const int row = p * 128 + w * 32 + (j & 3) + 8 * (j >> 2) + 4 * hf;
    if (r < 28) o[(size_t)row * D_ + 28 * h + r] = __builtin_bit_cast(u16, (__bf16)(c[j] / den));
  }
}

__global__ __launch_bounds__(256, 1) void k3(const u16* __restrict__ o, const u16* __restrict__ wo, const u16* __restrict__ x, u16* __restrict__ hb) {
  __shared__ __attribute__((aligned(16))) char lds[2 * 448 * 64];
  const int b = blockIdx.x, xcd = b & 7, j7 = b >> 3, rg = 4 * xcd + j7 / 7, cg = j7 % 7;
  const int tid = threadIdx.x, lane = tid & 63, w = tid >> 6, r = lane & 31, hf = lane >> 5, st = w & 1, ct = w >> 1;
  bf16x8 a[28];
  const u16* orow = o + (size_t)(rg * 64 + st * 32 + r) * D_ + 8 * hf;
#pragma unroll
  for (int k = 0; k < 28; ++k) a[k] = *(const bf16x8*)(orow + 16 * k);
  {
    const int c = tid & 7, k0 = tid >> 3;
    u32x4 v[14];
#pragma unroll
    for (int n = 0; n < 14; ++n) v[n] = *(const u32x4*)(wo + (size_t)(k0 + 32 * n) * D_ + cg * 64 + c * 8);
#pragma unroll
    for (int n = 0; n < 14; ++n) *(u32x4*)(lds + ((c >> 2) * 448 + k0 + 32 * n) * 64 + (c & 3) * 16) = v[n];
  }
  __syncthreads();
  f32x16 c = {0};
#pragma unroll
  for (int k = 0; k < 28; ++k) c = mfma(a[k], bfrag(lds + ct * 448 * 64, k, lane), c);
#pragma unroll
  for (int j = 0; j < 16; ++j) {
    const size_t idx = (size_t)(rg * 64 + st * 32 + (j & 3) + 8 * (j >> 2) + 4 * hf) * D_ + cg * 64 + ct * 32 + r;
    hb[idx] = __builtin_bit_cast(u16, (__bf16)(bf2f(__builtin_bit_cast(__bf16, x[idx])) + (float)(__bf16)c[j]));
  }
}

__global__ __launch_bounds__(256, 1) void k4(const u16* __restrict__ hb, const u16* __restrict__ wg, const u16* __restrict__ wu, u16* __restrict__ hh) {
  __shared__ __attribute__((aligned(16))) char lds[4 * 448 * 64];
  const int b = blockIdx.x, RG = b & 7, fc = b >> 3;
  const int tid = threadIdx.x, lane = tid & 63, w = tid >> 6, r = lane & 31, hf = lane >> 5;
  {
    const int c = tid & 7, k0 = tid >> 3;
    u32x4 v[28];
#pragma unroll
    for (int n = 0; n < 28; ++n) v[n] = *(const u32x4*)((n >= 14 ? wu : wg) + (size_t)(k0 + 32 * (n % 14)) * F_ + fc * 64 + c * 8);
#pragma unroll
    for (int n = 0; n < 28; ++n) *(u32x4*)(lds + (((n / 14) * 2 + (c >> 2)) * 448 + k0 + 32 * (n % 14)) * 64 + (c & 3) * 16) = v[n];
  }
  __syncthreads();
#pragma unroll 1
  for (int half = 0; half < 2; ++half) {
    bf16x8 a[28];
    const int row0 = RG * 256 + w * 64 + half * 32;
    const u16* hr = hb + (size_t)(row0 + r) * D_ + 8 * hf;
#pragma unroll
    for (int k = 0; k < 28; ++k) a[k] = *(const bf16x8*)(hr + 16 * k);
    layernorm_frags<28>(a);
    f32x16 g0 = {0}, g1 = {0}, u0 = {0}, u1 = {0};
#pragma unroll
    for (int k = 0; k < 28; ++k) {
      g0 = mfma(bfrag(lds, k, lane), a[k], g0);
      g1 = mfma(bfrag(lds + 448 * 64, k, lane), a[k], g1);
      u0 = mfma(bfrag(lds + 2 * 448 * 64, k, lane), a[k], u0);
      u1 = mfma(bfrag(lds + 3 * 448 * 64, k, lane), a[k], u1);
    }
    u16* dst = hh + (size_t)(row0 + r) * F_ + fc * 64 + 4 * hf;
#pragma unroll
    for (int q = 0; q < 4; ++q) {
      bf16x4 t0, t1;
#pragma unroll
      for (int u = 0; u < 4; ++u) {
        t0[u] = (__bf16)((float)(__bf16)g0[4 * q + u] * (float)(__bf16)u0[4 * q + u]);
        t1[u] = (__bf16)((float)(__bf16)g1[4 * q + u] * (float)(__bf16)u1[4 * q + u]);
      }
      *(bf16x4*)(dst + 8 * q) = t0;
      *(bf16x4*)(dst + 32 + 8 * q) = t1;
    }
  }
}

__global__ __launch_bounds__(256, 1) void k5(const u16* __restrict__ hh, const u16* __restrict__ wd, const u16* __restrict__ hb, u16* __restrict__ out) {
  __shared__ __attribute__((aligned(16))) char lds[4 * 2 * 224 * 64];
  const int b = blockIdx.x, xcd = b & 7, j7 = b >> 3, rg = 4 * xcd + j7 / 7, cg = j7 % 7;
  const int tid = threadIdx.x, lane = tid & 63, w = tid >> 6, r = lane & 31, hf = lane >> 5;
  f32x16 c00 = {0}, c01 = {0}, c10 = {0}, c11 = {0};
  char* my = lds + w * 2 * 224 * 64;
#pragma unroll 1
  for (int half = 0; half < 2; ++half) {
    const int k0 = w * 448 + half * 224;
    bf16x8 a0[14], a1[14];
    const u16* hr = hh + (size_t)(rg * 64 + r) * F_ + k0 + 8 * hf;
#pragma unroll
    for (int k = 0; k < 14; ++k) { a0[k] = *(const bf16x8*)(hr + 16 * k); a1[k] = *(const bf16x8*)(hr + 32 * F_ + 16 * k); }
    if (half) __syncthreads();
    {
      const int c = lane & 7, kk = lane >> 3;
#pragma unroll
      for (int m = 0; m < 2; ++m) {
        u32x4 v[14];
#pragma unroll
        for (int n = 0; n < 14; ++n) v[n] = *(const u32x4*)(wd + (size_t)(k0 + kk + 8 * (n + 14 * m)) * D_ + cg * 64 + c * 8);
#pragma unroll
        for (int n = 0; n < 14; ++n) *(u32x4*)(my + ((c >> 2) * 224 + kk + 8 * (n + 14 * m)) * 64 + (c & 3) * 16) = v[n];
      }
    }
    __syncthreads();
#pragma unroll
    for (int k = 0; k < 14; ++k) {
      bf16x8 b0 = bfrag(my, k, lane), b1 = bfrag(my + 224 * 64, k, lane);
      c00 = mfma(a0[k], b0, c00); c01 = mfma(a0[k], b1, c01);
      c10 = mfma(a1[k], b0, c10); c11 = mfma(a1[k], b1, c11);
    }
  }
  __syncthreads();
  float* red = (float*)lds;
#pragma unroll
  for (int j = 0; j < 16; ++j) {
    red[(w * 4 + 0) * 1024 + j * 64 + lane] = c00[j];
    red[(w * 4 + 1) * 1024 + j * 64 + lane] = c01[j];
    red[(w * 4 + 2) * 1024 + j * 64 + lane] = c10[j];
    red[(w * 4 + 3) * 1024 + j * 64 + lane] = c11[j];
  }
  __syncthreads();
  const int st = w >> 1, ct = w & 1;
#pragma unroll
  for (int j = 0; j < 16; ++j) {
    const float s = red[(0 + w) * 1024 + j * 64 + lane] + red[(4 + w) * 1024 + j * 64 + lane] + red[(8 + w) * 1024 + j * 64 + lane] + red[(12 + w) * 1024 + j * 64 + lane];
    const size_t idx = (size_t)(rg * 64 + st * 32 + (j & 3) + 8 * (j >> 2) + 4 * hf) * D_ + cg * 64 + ct * 32 + r;
    out[idx] = __builtin_bit_cast(u16, (__bf16)(bf2f(__builtin_bit_cast(__bf16, hb[idx])) + (float)(__bf16)s));
  }
}

extern "C" void kernel_launch(void* const* d_in, const int* in_sizes, int n_in, void* d_out, int out_size, void* d_ws, size_t ws_size, hipStream_t stream) {
  const u16* x = (const u16*)d_in[0]; const u16* wqkv = (const u16*)d_in[1]; const u16* wo = (const u16*)d_in[2];
  const u16* wg = (const u16*)d_in[4]; const u16* wu = (const u16*)d_in[5]; const u16* wd = (const u16*)d_in[6];
  char* ws = (char*)d_ws;
  u16* pq = (u16*)(ws + 0x000000); float* part = (float*)(ws + 0x200000); u16* o = (u16*)(ws + 0x400000); u16* hb = (u16*)(ws + 0x600000); u16* hh = (u16*)(ws + 0x800000);
  k1<<<256, 256, 0, stream>>>(x, wqkv, pq, part);
  k2<<<256, 256, 0, stream>>>(pq, part, o);
  k3<<<224, 256, 0, stream>>>(o, wo, x, hb);
  k4<<<224, 256, 0, stream>>>(hb, wg, wu, hh);
  k5<<<224, 256, 0, stream>>>(hh, wd, hb, (u16*)d_out);
}
```

```cpp
#include <hip/hip_runtime.h>
#include <stdint.h>

__global__ __launch_bounds__(256, 1) void fused_block(const void* x, const void* wqkv, const void* wo, const void* wg, const void* wu,
                                                      const void* wd, void* out, void* ws) {
  __shared__ __attribute__((aligned(16))) char lds[163840];
  const unsigned tid = threadIdx.x;
  asm volatile(
    "s_load_dwordx16 s[60:75], %0, 0\n"
    "v_and_b32 v22, 63, v0\n"
    "v_lshrrev_b32 v23, 6, v0\n"
    "s_getpc_b64 s[12:13]\n"
    "L_pc_%=:\n"
    "v_readfirstlane_b32 s48, v23\n"
    "s_lshr_b32 s55, %1, 3\n"
    "v_lshlrev_b32 v1, 4, v22\n"
    "s_lshl_b32 s16, s55, 2\n"
    "s_add_u32 s16, s16, s48\n"
    "s_and_b32 s16, s16, 63\n"
    "s_lshl_b32 s16, s16, 10\n"
    "s_and_b32 s13, s13, 0xffff\n"
    "s_mov_b32 s14, L_end_%=-L_pc_%=\n"
    "s_mov_b32 s15, 0x20000\n"
    "buffer_load_dwordx4 a[64:67], v1, s[12:15], s16 offen\n"
    "s_and_b32 s83, %1, 7\n"
    "s_and_b32 s49, s83, 1\n"
    "s_lshl_b32 s49, s49, 3\n"
    "s_lshr_b32 s82, s55, 2\n"
    "s_add_u32 s49, s49, s82\n"
    "s_lshr_b32 s50, s83, 1\n"
    "s_lshl_b32 s50, s50, 2\n"
    "s_and_b32 s82, s55, 3\n"
    "s_add_u32 s50, s50, s82\n"
    "s_lshl_b32 s80, s50, 1\n"
    "s_add_u32 s80, s80, s49\n"
    "s_mul_i32 s82, s80, 37\n"
    "s_lshr_b32 s82, s82, 8\n"
    "s_mul_i32 s82, s82, 7\n"
    "s_sub_u32 s80, s80, s82\n"
    "v_lshrrev_b32 v23, 3, v22\n"
    "v_and_b32 v24, 7, v22\n"
    "v_lshrrev_b32 v25, 4, v22\n"
    "v_and_b32 v26, 31, v22\n"
    "v_lshrrev_b32 v27, 5, v22\n"
    "v_xor_b32 v4, v24, v25\n"
    "v_lshlrev_b32 v4, 4, v4\n"
    "v_mul_u32_u24 v5, 896, v23\n"
    "v_add_u32 v4, v4, v5\n"
    "v_xor_b32 v5, 64, v4\n"
    "v_add_u32 v5, 6144, v5\n"
    "v_add_u32 v6, 12288, v4\n"
    "v_add_u32 v7, 12288, v5\n"
    "s_mul_i32 s82, s49, 56\n"
    "s_and_b32 s81, s49, 1\n"
    "s_lshl_b32 s81, s81, 3\n"
    "s_sub_u32 s82, s82, s81\n"
    "s_add_u32 s82, s82, 896\n"
    "v_lshrrev_b32 v8, 2, v22\n"
    "v_mul_u32_u24 v8, 2688, v8\n"
    "v_and_b32 v9, 3, v22\n"
    "v_lshl_add_u32 v8, v9, 4, v8\n"
    "v_add_u32 v8, s82, v8\n"
    "v_subrev_u32 v9, 128, v8\n"
    "s_lshl_b32 s76, s50, 2\n"
    "s_add_u32 s76, s76, s48\n"
    "s_mul_i32 s76, s76, 28672\n"
    "s_add_u32 s77, s76, 896\n"
    "s_lshl_b32 s4, s80, 7\n"
    "s_add_u32 s4, s4, s76\n"
    "s_mul_i32 s98, s48, 43008\n"
    "s_add_u32 s99, s98, 1204224\n"
    "s_mul_i32 s5, s80, 172032\n"
    "s_add_u32 s5, s5, s98\n"
    "s_lshl_b32 s91, s48, 12\n"
    "s_lshl_b32 s58, s48, 11\n"
    "s_cmp_lt_u32 s55, 28\n"
    "s_cselect_b32 s82, s55, 0\n"
    "s_mul_i32 s51, s82, 37\n"
    "s_lshr_b32 s51, s51, 8\n"
    "s_mul_i32 s52, s51, 7\n"
    "s_sub_u32 s52, s82, s52\n"
    "s_and_b32 s53, s83, 4\n"
    "s_add_u32 s53, s53, s51\n"
    "s_and_b32 s82, s83, 3\n"
    "s_mul_i32 s82, s82, 7\n"
    "s_add_u32 s54, s82, s52\n"
    "s_add_u32 s19, s51, s52\n"
    "s_and_b32 s19, s19, 3\n"
    "s_lshl_b32 s82, s83, 2\n"
    "s_add_u32 s51, s51, s82\n"
    "v_accvgpr_write_b32 a88, -1\n"
    "v_accvgpr_write_b32 a89, -1\n"
    "v_accvgpr_write_b32 a90, -1\n"
    "v_accvgpr_write_b32 a91, -1\n"
    "s_waitcnt lgkmcnt(0)\n"
    "s_mov_b32 s20, s60\n"
    "s_and_b32 s21, s61, 0xffff\n"
    "s_mov_b32 s22, 1835008\n"
    "s_mov_b32 s23, 0x20000\n"
    "s_mov_b32 s24, s62\n"
    "s_and_b32 s25, s63, 0xffff\n"
    "s_mov_b32 s26, 1204224\n"
    "s_mov_b32 s27, 0x20000\n"
    "s_add_u32 m0, s91, 139264\n"
    "s_nop 0\n"
    "buffer_load_dwordx4 v4, s[20:23], s4 offen offset:0 lds\n"
    "buffer_load_dwordx4 v5, s[20:23], s4 offen offset:1024 lds\n"
    "buffer_load_dwordx4 v6, s[20:23], s4 offen offset:2048 lds\n"
    "buffer_load_dwordx4 v7, s[20:23], s4 offen offset:3072 lds\n"
    "s_add_u32 s4, s4, 128\n"
    "s_cmp_eq_u32 s4, s77\n"
    "s_cselect_b32 s4, s76, s4\n"
    "s_add_u32 m0, s58, 0\n"
    "s_nop 0\n"
    "buffer_load_dwordx4 v8, s[24:27], s5 offen lds\n"
    "buffer_load_dwordx4 v9, s[24:27], s5 offen offset:1024 lds\n"
    "s_add_u32 s5, s5, 172032\n"
    "s_cmp_eq_u32 s5, s99\n"
    "s_cselect_b32 s5, s98, s5\n"
    "s_load_dword s56, s[74:75], 0\n"
    "s_and_b32 s84, s51, 3\n"
    "s_lshl_b32 s84, s84, 1\n"
    "s_add_u32 s84, s84, s52\n"
    "s_mul_i32 s82, s84, 37\n"
    "s_lshr_b32 s82, s82, 8\n"
    "s_mul_i32 s82, s82, 7\n"
    "s_sub_u32 s84, s84, s82\n"
    "s_lshl_b32 s84, s84, 12\n"
    "s_and_b32 s82, %1, 7\n"
    "s_lshl_b32 s82, s82, 2\n"
    "s_add_u32 s82, s82, s55\n"
    "s_sub_u32 s82, s82, 28\n"
    "s_cmp_ge_u32 s82, 14\n"
    "s_cselect_b32 s59, 14, 0\n"
    "s_cselect_b32 s57, 12288, 0\n"
    "s_sub_u32 s82, s82, s59\n"
    "s_cmp_ge_u32 s82, 14\n"
    "s_cselect_b32 s59, 14, 0\n"
    "s_cselect_b32 s57, 24576, s57\n"
    "s_sub_u32 s82, s82, s59\n"
    "s_lshl_b32 s82, s82, 1\n"
    "s_lshr_b32 s59, s48, 1\n"
    "s_add_u32 s82, s82, s59\n"
    "s_cmp_ge_u32 s55, 28\n"
    "s_cselect_b32 s54, s82, s54\n"
    "s_cselect_b32 s84, s57, s84\n"
    "s_add_u32 m0, s91, 57344\n"
    "s_nop 0\n"
    "buffer_load_dwordx4 v4, s[20:23], s4 offen offset:0 lds\n"
    "buffer_load_dwordx4 v5, s[20:23], s4 offen offset:1024 lds\n"
    "s_add_u32 s85, s84, 4096\n"
    "s_cmp_eq_u32 s85, 28672\n"
    "s_cselect_b32 s85, 0, s85\n"
    "s_add_u32 s86, s85, 4096\n"
    "s_cmp_eq_u32 s86, 28672\n"
    "s_cselect_b32 s86, 0, s86\n"
    "s_add_u32 s87, s86, 4096\n"
    "s_cmp_eq_u32 s87, 28672\n"
    "s_cselect_b32 s87, 0, s87\n"
    "s_add_u32 s88, s87, 4096\n"
    "s_cmp_eq_u32 s88, 28672\n"
    "s_cselect_b32 s88, 0, s88\n"
    "s_add_u32 s89, s88, 4096\n"
    "s_cmp_eq_u32 s89, 28672\n"
    "s_cselect_b32 s89, 0, s89\n"
    "s_add_u32 s90, s89, 4096\n"
    "s_cmp_eq_u32 s90, 28672\n"
    "s_cselect_b32 s90, 0, s90\n"
    "buffer_load_dwordx4 v6, s[20:23], s4 offen offset:2048 lds\n"
    "buffer_load_dwordx4 v7, s[20:23], s4 offen offset:3072 lds\n"
    "s_add_u32 s4, s4, 128\n"
    "s_cmp_eq_u32 s4, s77\n"
    "s_cselect_b32 s4, s76, s4\n"
    "s_mov_b32 s28, s64\n"
    "s_and_b32 s29, s65, 0xffff\n"
    "s_mov_b32 s30, 401408\n"
    "s_mov_b32 s31, 0x20000\n"
    "s_mov_b32 s36, s70\n"
    "s_and_b32 s37, s71, 0xffff\n"
    "s_mov_b32 s38, 1605632\n"
    "s_mov_b32 s39, 0x20000\n"
    "s_add_u32 m0, s91, 73728\n"
    "s_nop 0\n"
    "buffer_load_dwordx4 v4, s[20:23], s4 offen offset:0 lds\n"
    "buffer_load_dwordx4 v5, s[20:23], s4 offen offset:1024 lds\n"
    "s_mov_b32 s40, s72\n"
    "s_and_b32 s41, s73, 0xffff\n"
    "s_mov_b32 s42, 1835008\n"
    "s_mov_b32 s43, 0x20000\n"
    "s_mov_b32 s44, s74\n"
    "s_and_b32 s45, s75, 0xffff\n"
    "s_mov_b32 s46, 16777216\n"
    "s_mov_b32 s47, 0x20000\n"
    "buffer_load_dwordx4 v6, s[20:23], s4 offen offset:2048 lds\n"
    "buffer_load_dwordx4 v7, s[20:23], s4 offen offset:3072 lds\n"
    "s_add_u32 s4, s4, 128\n"
    "s_cmp_eq_u32 s4, s77\n"
    "s_cselect_b32 s4, s76, s4\n"
    "s_cmp_ge_u32 s48, 2\n"
    "s_cselect_b32 s66, s68, s66\n"
    "s_cselect_b32 s67, s69, s67\n"
    "s_mov_b32 s32, s66\n"
    "s_and_b32 s33, s67, 0xffff\n"
    "s_mov_b32 s34, 1605632\n"
    "s_mov_b32 s35, 0x20000\n"
    "s_cmp_ge_u32 s55, 28\n"
    "s_cselect_b32 s32, s36, s32\n"
    "s_cselect_b32 s33, s37, s33\n"
    "s_lshl_b32 s72, s51, 1\n"
    "s_add_u32 s72, s72, 1\n"
    "s_mul_i32 s72, s72, 28\n"
    "s_lshl_b32 s69, s52, 2\n"
    "s_add_u32 s72, s72, s69\n"
    "s_add_u32 s72, s72, s48\n"
    "s_lshl_b32 s72, s72, 12\n"
    "s_add_u32 s72, s72, 6291456\n"
    "s_cmp_lt_u32 s55, 28\n"
    "s_cselect_b32 s72, s72, 2147483392\n"
    "s_add_u32 m0, s58, 8192\n"
    "s_nop 0\n"
    "buffer_load_dwordx4 v8, s[24:27], s5 offen lds\n"
    "buffer_load_dwordx4 v9, s[24:27], s5 offen offset:1024 lds\n"
    "s_add_u32 s5, s5, 172032\n"
    "s_cmp_eq_u32 s5, s99\n"
    "s_cselect_b32 s5, s98, s5\n"
    "v_lshrrev_b32 v28, 1, v26\n"
    "v_and_b32 v28, 7, v28\n"
    "v_xor_b32 v28, v28, v27\n"
    "v_lshlrev_b32 v28, 4, v28\n"
    "v_lshl_add_u32 v28, v26, 7, v28\n"
    "v_add_u32 v28, s91, v28\n"
    "v_add_u32 v10, 57344, v28\n"
    "v_xor_b32 v11, 32, v10\n"
    "v_xor_b32 v12, 64, v10\n"
    "v_xor_b32 v13, 96, v10\n"
    "v_add_u32 v40, 65536, v10\n"
    "v_add_u32 v41, 65536, v11\n"
    "v_add_u32 v42, 65536, v12\n"
    "v_add_u32 v43, 65536, v13\n"
    "v_bfe_u32 v28, v22, 2, 2\n"
    "v_lshl_add_u32 v28, v27, 3, v28\n"
    "v_lshlrev_b32 v28, 6, v28\n"
    "v_and_b32 v29, 1, v25\n"
    "v_lshl_add_u32 v28, v29, 5, v28\n"
    "v_and_b32 v29, 3, v22\n"
    "v_lshl_add_u32 v28, v29, 3, v28\n"
    "v_add_u32 v14, s81, v28\n"
    "s_mul_i32 s82, s81, 12\n"
    "s_add_u32 s82, s82, 848\n"
    "v_subrev_u32 v15, s82, v8\n"
    "v_add_u32 v23, s58, v14\n"
    "s_lshl_b32 s83, s48, 10\n"
    "s_mov_b32 s59, 0x3f803f80\n"
    "s_mov_b32 s80, 0x358637bd\n"
    "s_mov_b32 s81, 0x3fb8aa3b\n"
    "v_mov_b32 v16, 0\n"
    "v_mov_b32 v17, 0\n"
    "v_mov_b32 v20, 0\n"
    "v_mov_b32 v21, 0\n"
    "v_mov_b32 v28, 2147483392\n"
    "v_cmp_eq_u32 vcc, 0, v0\n"
    "s_nop 1\n"
    "v_cndmask_b32 v2, v28, v22, vcc\n"
    "s_waitcnt vmcnt(12)\n"
    "ds_read_b128 v[144:147], v40 offset:16384\n"
    "ds_read_b128 v[148:151], v41 offset:16384\n"
    "ds_read_b128 v[152:155], v42 offset:16384\n"
    "ds_read_b128 v[156:159], v43 offset:16384\n"
    "s_waitcnt vmcnt(10)\n"
    "s_barrier\n"
    "ds_read_b64_tr_b16 v[32:33], v23 offset:0\n"
    "ds_read_b64_tr_b16 v[34:35], v23 offset:256\n"
    "ds_read_b64_tr_b16 v[36:37], v23 offset:1024\n"
    "ds_read_b64_tr_b16 v[38:39], v23 offset:1280\n"
    "ds_read_b64_tr_b16 a[0:1], v14 offset:0\n"
    "ds_read_b64_tr_b16 a[2:3], v14 offset:256\n"
    "ds_read_b64_tr_b16 a[4:5], v14 offset:1024\n"
    "ds_read_b64_tr_b16 a[6:7], v14 offset:1280\n"
    "ds_read_b64_tr_b16 a[8:9], v14 offset:2048\n"
    "ds_read_b64_tr_b16 a[10:11], v14 offset:2304\n"
    "ds_read_b64_tr_b16 a[12:13], v14 offset:3072\n"
    "ds_read_b64_tr_b16 a[14:15], v14 offset:3328\n"
    "ds_read_b64_tr_b16 a[16:17], v14 offset:4096\n"
    "ds_read_b64_tr_b16 a[18:19], v14 offset:4352\n"
    "ds_read_b64_tr_b16 a[20:21], v14 offset:5120\n"
    "ds_read_b64_tr_b16 a[22:23], v14 offset:5376\n"
    "ds_read_b64_tr_b16 a[24:25], v14 offset:6144\n"
    "ds_read_b64_tr_b16 a[26:27], v14 offset:6400\n"
    "ds_read_b64_tr_b16 a[28:29], v14 offset:7168\n"
    "ds_read_b64_tr_b16 a[30:31], v14 offset:7424\n"
    "s_waitcnt lgkmcnt(14)\n"
    "v_dot2c_f32_bf16 v16, s59, v144\n"
    "v_dot2c_f32_bf16 v17, v144, v144\n"
    "s_add_u32 m0, s91, 90112\n"
    "s_nop 0\n"
    "buffer_load_dwordx4 v4, s[20:23], s4 offen offset:0 lds\n"
    "v_dot2c_f32_bf16 v16, s59, v145\n"
    "v_dot2c_f32_bf16 v17, v145, v145\n"
    "v_dot2c_f32_bf16 v16, s59, v146\n"
    "v_dot2c_f32_bf16 v17, v146, v146\n"
    "v_dot2c_f32_bf16 v16, s59, v147\n"
    "v_dot2c_f32_bf16 v17, v147, v147\n"
    "v_dot2c_f32_bf16 v16, s59, v148\n"
    "v_dot2c_f32_bf16 v17, v148, v148\n"
    "v_dot2c_f32_bf16 v16, s59, v149\n"
    "v_dot2c_f32_bf16 v17, v149, v149\n"
    "v_dot2c_f32_bf16 v16, s59, v150\n"
    "v_dot2c_f32_bf16 v17, v150, v150\n"
    "v_dot2c_f32_bf16 v16, s59, v151\n"
    "v_dot2c_f32_bf16 v17, v151, v151\n"
    "s_waitcnt vmcnt(7)\n"
    "ds_read_b128 v[160:163], v10 offset:0\n"
    "ds_read_b128 v[164:167], v11 offset:0\n"
    "ds_read_b128 v[168:171], v12 offset:0\n"
    "ds_read_b128 v[172:175], v13 offset:0\n"
    "buffer_load_dwordx4 v5, s[20:23], s4 offen offset:1024 lds\n"
    "v_dot2c_f32_bf16 v16, s59, v152\n"
    "v_dot2c_f32_bf16 v17, v152, v152\n"
    "v_dot2c_f32_bf16 v16, s59, v153\n"
    "v_dot2c_f32_bf16 v17, v153, v153\n"
    "v_dot2c_f32_bf16 v16, s59, v154\n"
    "v_dot2c_f32_bf16 v17, v154, v154\n"
    "v_dot2c_f32_bf16 v16, s59, v155\n"
    "v_dot2c_f32_bf16 v17, v155, v155\n"
    "v_dot2c_f32_bf16 v16, s59, v156\n"
    "v_dot2c_f32_bf16 v17, v156, v156\n"
    "v_dot2c_f32_bf16 v16, s59, v157\n"
    "v_dot2c_f32_bf16 v17, v157, v157\n"
    "v_dot2c_f32_bf16 v16, s59, v158\n"
    "v_dot2c_f32_bf16 v17, v158, v158\n"
    "v_dot2c_f32_bf16 v16, s59, v159\n"
    "v_dot2c_f32_bf16 v17, v159, v159\n"
    "buffer_load_dwordx4 v6, s[20:23], s4 offen offset:2048 lds\n"
    "s_waitcnt lgkmcnt(3)\n"
    "v_dot2c_f32_bf16 v16, s59, v160\n"
    "v_dot2c_f32_bf16 v17, v160, v160\n"
    "v_dot2c_f32_bf16 v16, s59, v161\n"
    "v_dot2c_f32_bf16 v17, v161, v161\n"
    "v_dot2c_f32_bf16 v16, s59, v162\n"
    "v_dot2c_f32_bf16 v17, v162, v162\n"
    "v_dot2c_f32_bf16 v16, s59, v163\n"
    "v_dot2c_f32_bf16 v17, v163, v163\n"
    "s_waitcnt lgkmcnt(2)\n"
    "v_dot2c_f32_bf16 v16, s59, v164\n"
    "v_dot2c_f32_bf16 v17, v164, v164\n"
    "v_dot2c_f32_bf16 v16, s59, v165\n"
    "v_dot2c_f32_bf16 v17, v165, v165\n"
    "v_dot2c_f32_bf16 v16, s59, v166\n"
    "v_dot2c_f32_bf16 v17, v166, v166\n"
    "v_dot2c_f32_bf16 v16, s59, v167\n"
    "v_dot2c_f32_bf16 v17, v167, v167\n"
    "buffer_load_dwordx4 v7, s[20:23], s4 offen offset:3072 lds\n"
    "s_add_u32 s4, s4, 128\n"
    "s_cmp_eq_u32 s4, s77\n"
    "s_cselect_b32 s4, s76, s4\n"
    "s_waitcnt lgkmcnt(1)\n"
    "v_dot2c_f32_bf16 v16, s59, v168\n"
    "v_dot2c_f32_bf16 v17, v168, v168\n"
    "v_dot2c_f32_bf16 v16, s59, v169\n"
    "v_dot2c_f32_bf16 v17, v169, v169\n"
    "v_dot2c_f32_bf16 v16, s59, v170\n"
    "v_dot2c_f32_bf16 v17, v170, v170\n"
    "v_dot2c_f32_bf16 v16, s59, v171\n"
    "v_dot2c_f32_bf16 v17, v171, v171\n"
    "s_waitcnt lgkmcnt(0)\n"
    "v_dot2c_f32_bf16 v16, s59, v172\n"
    "v_dot2c_f32_bf16 v17, v172, v172\n"
    "v_dot2c_f32_bf16 v16, s59, v173\n"
    "v_dot2c_f32_bf16 v17, v173, v173\n"
    "v_dot2c_f32_bf16 v16, s59, v174\n"
    "v_dot2c_f32_bf16 v17, v174, v174\n"
    "v_dot2c_f32_bf16 v16, s59, v175\n"
    "v_dot2c_f32_bf16 v17, v175, v175\n"
    "v_dot2c_f32_bf16 v20, s59, v32\n"
    "v_dot2c_f32_bf16 v21, s59, v36\n"
    "v_dot2c_f32_bf16 v20, s59, v33\n"
    "v_dot2c_f32_bf16 v21, s59, v37\n"
    "v_dot2c_f32_bf16 v20, s59, v34\n"
    "v_dot2c_f32_bf16 v21, s59, v38\n"
    "v_dot2c_f32_bf16 v20, s59, v35\n"
    "v_dot2c_f32_bf16 v21, s59, v39\n"
    "v_mfma_f32_32x32x16_bf16 v[80:95], v[144:147], a[0:3], 0\n"
    "s_waitcnt vmcnt(6)\n"
    "ds_read_b128 v[176:179], v10 offset:16384\n"
    "v_mfma_f32_32x32x16_bf16 v[96:111], v[144:147], a[4:7], 0\n"
    "ds_read_b128 v[180:183], v11 offset:16384\n"
    "ds_read_b128 v[184:187], v12 offset:16384\n"
    "s_add_u32 m0, s58, 16384\n"
    "s_nop 0\n"
    "buffer_load_dwordx4 v8, s[24:27], s5 offen lds\n"
    "v_mfma_f32_32x32x16_bf16 v[80:95], v[148:151], a[8:11], v[80:95]\n"
    "ds_read_b128 v[188:191], v13 offset:16384\n"
    "v_mfma_f32_32x32x16_bf16 v[96:111], v[148:151], a[12:15], v[96:111]\n"
    "s_waitcnt vmcnt(5)\n"
    "s_barrier\n"
    "ds_read_b64_tr_b16 v[32:33], v23 offset:8192\n"
    "ds_read_b64_tr_b16 v[34:35], v23 offset:8448\n"
    "ds_read_b64_tr_b16 v[36:37], v23 offset:9216\n"
    "ds_read_b64_tr_b16 v[38:39], v23 offset:9472\n"
    "buffer_load_dwordx4 v9, s[24:27], s5 offen offset:1024 lds\n"
    "s_add_u32 s5, s5, 172032\n"
    "s_cmp_eq_u32 s5, s99\n"
    "s_cselect_b32 s5, s98, s5\n"
    "v_mfma_f32_32x32x16_bf16 v[80:95], v[152:155], a[16:19], v[80:95]\n"
    "ds_read_b64_tr_b16 a[32:33], v14 offset:8192\n"
    "ds_read_b64_tr_b16 a[34:35], v14 offset:8448\n"
    "ds_read_b64_tr_b16 a[36:37], v14 offset:9216\n"
    "ds_read_b64_tr_b16 a[38:39], v14 offset:9472\n"
    "v_mfma_f32_32x32x16_bf16 v[96:111], v[152:155], a[20:23], v[96:111]\n"
    "ds_read_b64_tr_b16 a[40:41], v14 offset:10240\n"
    "ds_read_b64_tr_b16 a[42:43], v14 offset:10496\n"
    "ds_read_b64_tr_b16 a[44:45], v14 offset:11264\n"
    "ds_read_b64_tr_b16 a[46:47], v14 offset:11520\n"
    "s_add_u32 m0, s91, 106496\n"
    "s_nop 0\n"
    "buffer_load_dwordx4 v4, s[20:23], s4 offen offset:0 lds\n"
    "v_mfma_f32_32x32x16_bf16 v[80:95], v[156:159], a[24:27], v[80:95]\n"
    "ds_read_b64_tr_b16 a[48:49], v14 offset:12288\n"
    "ds_read_b64_tr_b16 a[50:51], v14 offset:12544\n"
    "ds_read_b64_tr_b16 a[52:53], v14 offset:13312\n"
    "ds_read_b64_tr_b16 a[54:55], v14 offset:13568\n"
    "v_mfma_f32_32x32x16_bf16 v[96:111], v[156:159], a[28:31], v[96:111]\n"
    "ds_read_b64_tr_b16 a[56:57], v14 offset:14336\n"
    "ds_read_b64_tr_b16 a[58:59], v14 offset:14592\n"
    "ds_read_b64_tr_b16 a[60:61], v14 offset:15360\n"
    "ds_read_b64_tr_b16 a[62:63], v14 offset:15616\n"
    "buffer_load_dwordx4 v5, s[20:23], s4 offen offset:1024 lds\n"
    "s_bitcmp1_b32 0x3ffc, s49\n"
    "s_cbranch_scc1 L_pace_1_%=\n"
    "s_sleep 2\n"
    "L_pace_1_%=:\n"
    "s_add_u32 m0, s91, 106496\n"
    "s_nop 0\n"
    "buffer_load_dwordx4 v6, s[20:23], s4 offen offset:2048 lds\n"
    "s_waitcnt lgkmcnt(14)\n"
    "v_dot2c_f32_bf16 v16, s59, v176\n"
    "v_dot2c_f32_bf16 v17, v176, v176\n"
    "v_dot2c_f32_bf16 v16, s59, v177\n"
    "v_dot2c_f32_bf16 v17, v177, v177\n"
    "v_dot2c_f32_bf16 v16, s59, v178\n"
    "v_dot2c_f32_bf16 v17, v178, v178\n"
    "v_dot2c_f32_bf16 v16, s59, v179\n"
    "v_dot2c_f32_bf16 v17, v179, v179\n"
    "v_dot2c_f32_bf16 v16, s59, v180\n"
    "v_dot2c_f32_bf16 v17, v180, v180\n"
    "v_dot2c_f32_bf16 v16, s59, v181\n"
    "v_dot2c_f32_bf16 v17, v181, v181\n"
    "v_dot2c_f32_bf16 v16, s59, v182\n"
    "v_dot2c_f32_bf16 v17, v182, v182\n"
    "v_dot2c_f32_bf16 v16, s59, v183\n"
    "v_dot2c_f32_bf16 v17, v183, v183\n"
    "buffer_load_dwordx4 v7, s[20:23], s4 offen offset:3072 lds\n"
    "s_add_u32 s4, s4, 128\n"
    "s_cmp_eq_u32 s4, s77\n"
    "s_cselect_b32 s4, s76, s4\n"
    "v_dot2c_f32_bf16 v16, s59, v184\n"
    "v_dot2c_f32_bf16 v17, v184, v184\n"
    "v_dot2c_f32_bf16 v16, s59, v185\n"
    "v_dot2c_f32_bf16 v17, v185, v185\n"
    "v_dot2c_f32_bf16 v16, s59, v186\n"
    "v_dot2c_f32_bf16 v17, v186, v186\n"
    "v_dot2c_f32_bf16 v16, s59, v187\n"
    "v_dot2c_f32_bf16 v17, v187, v187\n"
    "v_dot2c_f32_bf16 v16, s59, v188\n"
    "v_dot2c_f32_bf16 v17, v188, v188\n"
    "v_dot2c_f32_bf16 v16, s59, v189\n"
    "v_dot2c_f32_bf16 v17, v189, v189\n"
    "v_dot2c_f32_bf16 v16, s59, v190\n"
    "v_dot2c_f32_bf16 v17, v190, v190\n"
    "v_dot2c_f32_bf16 v16, s59, v191\n"
    "v_dot2c_f32_bf16 v17, v191, v191\n"
    "v_dot2c_f32_bf16 v20, s59, v32\n"
    "v_dot2c_f32_bf16 v21, s59, v36\n"
    "v_dot2c_f32_bf16 v20, s59, v33\n"
    "v_dot2c_f32_bf16 v21, s59, v37\n"
    "v_dot2c_f32_bf16 v20, s59, v34\n"
    "v_dot2c_f32_bf16 v21, s59, v38\n"
    "v_dot2c_f32_bf16 v20, s59, v35\n"
    "v_dot2c_f32_bf16 v21, s59, v39\n"
    "v_mfma_f32_32x32x16_bf16 v[80:95], v[160:163], a[32:35], v[80:95]\n"
    "s_waitcnt vmcnt(6)\n"
    "ds_read_b128 v[192:195], v10 offset:32768\n"
    "s_waitcnt lgkmcnt(13)\n"
    "v_mfma_f32_32x32x16_bf16 v[96:111], v[160:163], a[36:39], v[96:111]\n"
    "ds_read_b128 v[196:199], v11 offset:32768\n"
    "ds_read_b128 v[200:203], v12 offset:32768\n"
    "s_add_u32 m0, s58, 24576\n"
    "s_nop 0\n"
    "buffer_load_dwordx4 v8, s[24:27], s5 offen lds\n"
    "s_waitcnt lgkmcnt(13)\n"
    "v_mfma_f32_32x32x16_bf16 v[80:95], v[164:167], a[40:43], v[80:95]\n"
    "ds_read_b128 v[204:207], v13 offset:32768\n"
    "s_waitcnt lgkmcnt(12)\n"
    "v_mfma_f32_32x32x16_bf16 v[96:111], v[164:167], a[44:47], v[96:111]\n"
    "s_waitcnt vmcnt(5)\n"
    "s_barrier\n"
    "ds_read_b64_tr_b16 v[32:33], v23 offset:16384\n"
    "ds_read_b64_tr_b16 v[34:35], v23 offset:16640\n"
    "ds_read_b64_tr_b16 v[36:37], v23 offset:17408\n"
    "ds_read_b64_tr_b16 v[38:39], v23 offset:17664\n"
    "buffer_load_dwordx4 v9, s[24:27], s5 offen offset:1024 lds\n"
    "s_add_u32 s5, s5, 172032\n"
    "s_cmp_eq_u32 s5, s99\n"
    "s_cselect_b32 s5, s98, s5\n"
    "s_waitcnt lgkmcnt(14)\n"
    "v_mfma_f32_32x32x16_bf16 v[80:95], v[168:171], a[48:51], v[80:95]\n"
    "ds_read_b64_tr_b16 a[0:1], v14 offset:16384\n"
    "ds_read_b64_tr_b16 a[2:3], v14 offset:16640\n"
    "ds_read_b64_tr_b16 a[4:5], v14 offset:17408\n"
    "ds_read_b64_tr_b16 a[6:7], v14 offset:17664\n"
    "s_waitcnt lgkmcnt(14)\n"
    "v_mfma_f32_32x32x16_bf16 v[96:111], v[168:171], a[52:55], v[96:111]\n"
    "ds_read_b64_tr_b16 a[8:9], v14 offset:18432\n"
    "ds_read_b64_tr_b16 a[10:11], v14 offset:18688\n"
    "ds_read_b64_tr_b16 a[12:13], v14 offset:19456\n"
    "ds_read_b64_tr_b16 a[14:15], v14 offset:19712\n"
    "s_add_u32 m0, s91, 122880\n"
    "s_nop 0\n"
    "buffer_load_dwordx4 v4, s[20:23], s4 offen offset:0 lds\n"
    "v_mfma_f32_32x32x16_bf16 v[80:95], v[172:175], a[56:59], v[80:95]\n"
    "ds_read_b64_tr_b16 a[16:17], v14 offset:20480\n"
    "ds_read_b64_tr_b16 a[18:19], v14 offset:20736\n"
    "ds_read_b64_tr_b16 a[20:21], v14 offset:21504\n"
    "ds_read_b64_tr_b16 a[22:23], v14 offset:21760\n"
    "s_waitcnt lgkmcnt(14)\n"
    "v_mfma_f32_32x32x16_bf16 v[96:111], v[172:175], a[60:63], v[96:111]\n"
    "ds_read_b64_tr_b16 a[24:25], v14 offset:22528\n"
    "ds_read_b64_tr_b16 a[26:27], v14 offset:22784\n"
    "ds_read_b64_tr_b16 a[28:29], v14 offset:23552\n"
    "ds_read_b64_tr_b16 a[30:31], v14 offset:23808\n"
    "buffer_load_dwordx4 v5, s[20:23], s4 offen offset:1024 lds\n"
    "s_bitcmp1_b32 0x3ffc, s49\n"
    "s_cbranch_scc1 L_pace_2_%=\n"
    "s_sleep 2\n"
    "L_pace_2_%=:\n"
    "s_add_u32 m0, s91, 122880\n"
    "s_nop 0\n"
    "buffer_load_dwordx4 v6, s[20:23], s4 offen offset:2048 lds\n"
    "v_dot2c_f32_bf16 v16, s59, v192\n"
    "v_dot2c_f32_bf16 v17, v192, v192\n"
    "v_dot2c_f32_bf16 v16, s59, v193\n"
    "v_dot2c_f32_bf16 v17, v193, v193\n"
    "v_dot2c_f32_bf16 v16, s59, v194\n"
    "v_dot2c_f32_bf16 v17, v194, v194\n"
    "v_dot2c_f32_bf16 v16, s59, v195\n"
    "v_dot2c_f32_bf16 v17, v195, v195\n"
    "v_dot2c_f32_bf16 v16, s59, v196\n"
    "v_dot2c_f32_bf16 v17, v196, v196\n"
    "v_dot2c_f32_bf16 v16, s59, v197\n"
    "v_dot2c_f32_bf16 v17, v197, v197\n"
    "v_dot2c_f32_bf16 v16, s59, v198\n"
    "v_dot2c_f32_bf16 v17, v198, v198\n"
    "v_dot2c_f32_bf16 v16, s59, v199\n"
    "v_dot2c_f32_bf16 v17, v199, v199\n"
    "buffer_load_dwordx4 v7, s[20:23], s4 offen offset:3072 lds\n"
    "s_add_u32 s4, s4, 128\n"
    "s_cmp_eq_u32 s4, s77\n"
    "s_cselect_b32 s4, s76, s4\n"
    "v_dot2c_f32_bf16 v16, s59, v200\n"
    "v_dot2c_f32_bf16 v17, v200, v200\n"
    "v_dot2c_f32_bf16 v16, s59, v201\n"
    "v_dot2c_f32_bf16 v17, v201, v201\n"
    "v_dot2c_f32_bf16 v16, s59, v202\n"
    "v_dot2c_f32_bf16 v17, v202, v202\n"
    "v_dot2c_f32_bf16 v16, s59, v203\n"
    "v_dot2c_f32_bf16 v17, v203, v203\n"
    "v_dot2c_f32_bf16 v16, s59, v204\n"
    "v_dot2c_f32_bf16 v17, v204, v204\n"
    "v_dot2c_f32_bf16 v16, s59, v205\n"
    "v_dot2c_f32_bf16 v17, v205, v205\n"
    "v_dot2c_f32_bf16 v16, s59, v206\n"
    "v_dot2c_f32_bf16 v17, v206, v206\n"
    "v_dot2c_f32_bf16 v16, s59, v207\n"
    "v_dot2c_f32_bf16 v17, v207, v207\n"
    "v_dot2c_f32_bf16 v20, s59, v32\n"
    "s_waitcnt lgkmcnt(14)\n"
    "v_dot2c_f32_bf16 v21, s59, v36\n"
    "v_dot2c_f32_bf16 v20, s59, v33\n"
    "v_dot2c_f32_bf16 v21, s59, v37\n"
    "v_dot2c_f32_bf16 v20, s59, v34\n"
    "v_dot2c_f32_bf16 v21, s59, v38\n"
    "v_dot2c_f32_bf16 v20, s59, v35\n"
    "v_dot2c_f32_bf16 v21, s59, v39\n"
    "v_mfma_f32_32x32x16_bf16 v[80:95], v[176:179], a[0:3], v[80:95]\n"
    "s_waitcnt vmcnt(6)\n"
    "ds_read_b128 v[208:211], v10 offset:49152\n"
    "s_waitcnt lgkmcnt(13)\n"
    "v_mfma_f32_32x32x16_bf16 v[96:111], v[176:179], a[4:7], v[96:111]\n"
    "ds_read_b128 v[212:215], v11 offset:49152\n"
    "ds_read_b128 v[216:219], v12 offset:49152\n"
    "s_add_u32 m0, s58, 32768\n"
    "s_nop 0\n"
    "buffer_load_dwordx4 v8, s[24:27], s5 offen lds\n"
    "s_waitcnt lgkmcnt(13)\n"
    "v_mfma_f32_32x32x16_bf16 v[80:95], v[180:183], a[8:11], v[80:95]\n"
    "ds_read_b128 v[220:223], v13 offset:49152\n"
    "s_waitcnt lgkmcnt(12)\n"
    "v_mfma_f32_32x32x16_bf16 v[96:111], v[180:183], a[12:15], v[96:111]\n"
    "s_waitcnt vmcnt(5)\n"
    "s_barrier\n"
    "ds_read_b64_tr_b16 v[32:33], v23 offset:24576\n"
    "ds_read_b64_tr_b16 v[34:35], v23 offset:24832\n"
    "ds_read_b64_tr_b16 v[36:37], v23 offset:25600\n"
    "ds_read_b64_tr_b16 v[38:39], v23 offset:25856\n"
    "buffer_load_dwordx4 v9, s[24:27], s5 offen offset:1024 lds\n"
    "s_add_u32 s5, s5, 172032\n"
    "s_cmp_eq_u32 s5, s99\n"
    "s_cselect_b32 s5, s98, s5\n"
    "s_waitcnt lgkmcnt(14)\n"
    "v_mfma_f32_32x32x16_bf16 v[80:95], v[184:187], a[16:19], v[80:95]\n"
    "ds_read_b64_tr_b16 a[32:33], v14 offset:24576\n"
    "ds_read_b64_tr_b16 a[34:35], v14 offset:24832\n"
    "ds_read_b64_tr_b16 a[36:37], v14 offset:25600\n"
    "ds_read_b64_tr_b16 a[38:39], v14 offset:25856\n"
    "s_waitcnt lgkmcnt(14)\n"
    "v_mfma_f32_32x32x16_bf16 v[96:111], v[184:187], a[20:23], v[96:111]\n"
    "ds_read_b64_tr_b16 a[40:41], v14 offset:26624\n"
    "ds_read_b64_tr_b16 a[42:43], v14 offset:26880\n"
    "ds_read_b64_tr_b16 a[44:45], v14 offset:27648\n"
    "ds_read_b64_tr_b16 a[46:47], v14 offset:27904\n"
    "s_add_u32 m0, s91, 139264\n"
    "s_nop 0\n"
    "buffer_load_dwordx4 v4, s[20:23], s4 offen offset:0 lds\n"
    "v_mfma_f32_32x32x16_bf16 v[80:95], v[188:191], a[24:27], v[80:95]\n"
    "ds_read_b64_tr_b16 a[48:49], v14 offset:28672\n"
    "ds_read_b64_tr_b16 a[50:51], v14 offset:28928\n"
    "ds_read_b64_tr_b16 a[52:53], v14 offset:29696\n"
    "ds_read_b64_tr_b16 a[54:55], v14 offset:29952\n"
    "s_waitcnt lgkmcnt(14)\n"
    "v_mfma_f32_32x32x16_bf16 v[96:111], v[188:191], a[28:31], v[96:111]\n"
    "ds_read_b64_tr_b16 a[56:57], v14 offset:30720\n"
    "ds_read_b64_tr_b16 a[58:59], v14 offset:30976\n"
    "ds_read_b64_tr_b16 a[60:61], v14 offset:31744\n"
    "ds_read_b64_tr_b16 a[62:63], v14 offset:32000\n"
    "buffer_load_dwordx4 v5, s[20:23], s4 offen offset:1024 lds\n"
    "s_bitcmp1_b32 0x3ffc, s49\n"
    "s_cbranch_scc1 L_pace_3_%=\n"
    "s_sleep 2\n"
    "L_pace_3_%=:\n"
    "s_add_u32 m0, s91, 139264\n"
    "s_nop 0\n"
    "buffer_load_dwordx4 v6, s[20:23], s4 offen offset:2048 lds\n"
    "v_dot2c_f32_bf16 v16, s59, v208\n"
    "v_dot2c_f32_bf16 v17, v208, v208\n"
    "v_dot2c_f32_bf16 v16, s59, v209\n"
    "v_dot2c_f32_bf16 v17, v209, v209\n"
    "v_dot2c_f32_bf16 v16, s59, v210\n"
    "v_dot2c_f32_bf16 v17, v210, v210\n"
    "v_dot2c_f32_bf16 v16, s59, v211\n"
    "v_dot2c_f32_bf16 v17, v211, v211\n"
    "v_dot2c_f32_bf16 v16, s59, v212\n"
    "v_dot2c_f32_bf16 v17, v212, v212\n"
    "v_dot2c_f32_bf16 v16, s59, v213\n"
    "v_dot2c_f32_bf16 v17, v213, v213\n"
    "v_dot2c_f32_bf16 v16, s59, v214\n"
    "v_dot2c_f32_bf16 v17, v214, v214\n"
    "v_dot2c_f32_bf16 v16, s59, v215\n"
    "v_dot2c_f32_bf16 v17, v215, v215\n"
    "buffer_load_dwordx4 v7, s[20:23], s4 offen offset:3072 lds\n"
    "v_dot2c_f32_bf16 v16, s59, v216\n"
    "v_dot2c_f32_bf16 v17, v216, v216\n"
    "v_dot2c_f32_bf16 v16, s59, v217\n"
    "v_dot2c_f32_bf16 v17, v217, v217\n"
    "v_dot2c_f32_bf16 v16, s59, v218\n"
    "v_dot2c_f32_bf16 v17, v218, v218\n"
    "v_dot2c_f32_bf16 v16, s59, v219\n"
    "v_dot2c_f32_bf16 v17, v219, v219\n"
    "v_dot2c_f32_bf16 v16, s59, v220\n"
    "v_dot2c_f32_bf16 v17, v220, v220\n"
    "v_dot2c_f32_bf16 v16, s59, v221\n"
    "v_dot2c_f32_bf16 v17, v221, v221\n"
    "v_dot2c_f32_bf16 v16, s59, v222\n"
    "v_dot2c_f32_bf16 v17, v222, v222\n"
    "v_dot2c_f32_bf16 v16, s59, v223\n"
    "v_dot2c_f32_bf16 v17, v223, v223\n"
    "v_dot2c_f32_bf16 v20, s59, v32\n"
    "s_waitcnt lgkmcnt(14)\n"
    "v_dot2c_f32_bf16 v21, s59, v36\n"
    "v_dot2c_f32_bf16 v20, s59, v33\n"
    "v_dot2c_f32_bf16 v21, s59, v37\n"
    "v_dot2c_f32_bf16 v20, s59, v34\n"
    "v_dot2c_f32_bf16 v21, s59, v38\n"
    "v_dot2c_f32_bf16 v20, s59, v35\n"
    "v_dot2c_f32_bf16 v21, s59, v39\n"
    "v_mfma_f32_32x32x16_bf16 v[80:95], v[192:195], a[32:35], v[80:95]\n"
    "s_waitcnt vmcnt(6)\n"
    "ds_read_b128 v[224:227], v40 offset:0\n"
    "s_waitcnt lgkmcnt(13)\n"
    "v_mfma_f32_32x32x16_bf16 v[96:111], v[192:195], a[36:39], v[96:111]\n"
    "ds_read_b128 v[228:231], v41 offset:0\n"
    "ds_read_b128 v[232:235], v42 offset:0\n"
    "s_add_u32 m0, s58, 40960\n"
    "s_nop 0\n"
    "buffer_load_dwordx4 v8, s[24:27], s5 offen lds\n"
    "s_waitcnt lgkmcnt(13)\n"
    "v_mfma_f32_32x32x16_bf16 v[80:95], v[196:199], a[40:43], v[80:95]\n"
    "ds_read_b128 v[236:239], v43 offset:0\n"
    "s_waitcnt lgkmcnt(12)\n"
    "v_mfma_f32_32x32x16_bf16 v[96:111], v[196:199], a[44:47], v[96:111]\n"
    "s_waitcnt vmcnt(5)\n"
    "s_barrier\n"
    "ds_read_b64_tr_b16 v[32:33], v23 offset:32768\n"
    "ds_read_b64_tr_b16 v[34:35], v23 offset:33024\n"
    "ds_read_b64_tr_b16 v[36:37], v23 offset:33792\n"
    "ds_read_b64_tr_b16 v[38:39], v23 offset:34048\n"
    "buffer_load_dwordx4 v9, s[24:27], s5 offen offset:1024 lds\n"
    "s_add_u32 s5, s5, 172032\n"
    "s_cmp_eq_u32 s5, s99\n"
    "s_cselect_b32 s5, s98, s5\n"
    "s_waitcnt lgkmcnt(14)\n"
    "v_mfma_f32_32x32x16_bf16 v[80:95], v[200:203], a[48:51], v[80:95]\n"
    "ds_read_b64_tr_b16 a[0:1], v14 offset:32768\n"
    "ds_read_b64_tr_b16 a[2:3], v14 offset:33024\n"
    "ds_read_b64_tr_b16 a[4:5], v14 offset:33792\n"
    "ds_read_b64_tr_b16 a[6:7], v14 offset:34048\n"
    "s_waitcnt lgkmcnt(14)\n"
    "v_mfma_f32_32x32x16_bf16 v[96:111], v[200:203], a[52:55], v[96:111]\n"
    "ds_read_b64_tr_b16 a[8:9], v14 offset:34816\n"
    "ds_read_b64_tr_b16 a[10:11], v14 offset:35072\n"
    "ds_read_b64_tr_b16 a[12:13], v14 offset:35840\n"
    "ds_read_b64_tr_b16 a[14:15], v14 offset:36096\n"
    "s_add_u32 m0, s58, 49152\n"
    "s_nop 0\n"
    "buffer_load_dwordx4 v8, s[24:27], s5 offen lds\n"
    "v_mfma_f32_32x32x16_bf16 v[80:95], v[204:207], a[56:59], v[80:95]\n"
    "ds_read_b64_tr_b16 a[16:17], v14 offset:36864\n"
    "ds_read_b64_tr_b16 a[18:19], v14 offset:37120\n"
    "ds_read_b64_tr_b16 a[20:21], v14 offset:37888\n"
    "ds_read_b64_tr_b16 a[22:23], v14 offset:38144\n"
    "s_waitcnt lgkmcnt(14)\n"
    "v_mfma_f32_32x32x16_bf16 v[96:111], v[204:207], a[60:63], v[96:111]\n"
    "ds_read_b64_tr_b16 a[24:25], v14 offset:38912\n"
    "ds_read_b64_tr_b16 a[26:27], v14 offset:39168\n"
    "ds_read_b64_tr_b16 a[28:29], v14 offset:39936\n"
    "ds_read_b64_tr_b16 a[30:31], v14 offset:40192\n"
    "buffer_load_dwordx4 v9, s[24:27], s5 offen offset:1024 lds\n"
    "s_add_u32 s5, s5, 172032\n"
    "s_cmp_eq_u32 s5, s99\n"
    "s_cselect_b32 s5, s98, s5\n"
    "s_bitcmp1_b32 0x3ffc, s49\n"
    "s_cbranch_scc1 L_pace_4_%=\n"
    "s_sleep 2\n"
    "L_pace_4_%=:\n"
    "s_add_u32 m0, s83, 57344\n"
    "s_nop 0\n"
    "buffer_load_dwordx4 v15, s[24:27], s5 offen lds\n"
    "s_add_u32 s5, s5, 172032\n"
    "s_cmp_eq_u32 s5, s99\n"
    "s_cselect_b32 s5, s98, s5\n"
    "v_dot2c_f32_bf16 v16, s59, v224\n"
    "v_dot2c_f32_bf16 v17, v224, v224\n"
    "v_dot2c_f32_bf16 v16, s59, v225\n"
    "v_dot2c_f32_bf16 v17, v225, v225\n"
    "v_dot2c_f32_bf16 v16, s59, v226\n"
    "v_dot2c_f32_bf16 v17, v226, v226\n"
    "v_dot2c_f32_bf16 v16, s59, v227\n"
    "v_dot2c_f32_bf16 v17, v227, v227\n"
    "v_dot2c_f32_bf16 v16, s59, v228\n"
    "v_dot2c_f32_bf16 v17, v228, v228\n"
    "v_dot2c_f32_bf16 v16, s59, v229\n"
    "v_dot2c_f32_bf16 v17, v229, v229\n"
    "v_dot2c_f32_bf16 v16, s59, v230\n"
    "v_dot2c_f32_bf16 v17, v230, v230\n"
    "v_dot2c_f32_bf16 v16, s59, v231\n"
    "v_dot2c_f32_bf16 v17, v231, v231\n"
    "s_add_u32 m0, s83, 61440\n"
    "s_nop 0\n"
    "buffer_load_dwordx4 v15, s[24:27], s5 offen lds\n"
    "s_add_u32 s5, s5, 172032\n"
    "s_cmp_eq_u32 s5, s99\n"
    "s_cselect_b32 s5, s98, s5\n"
    "v_dot2c_f32_bf16 v16, s59, v232\n"
    "v_dot2c_f32_bf16 v17, v232, v232\n"
    "v_dot2c_f32_bf16 v16, s59, v233\n"
    "v_dot2c_f32_bf16 v17, v233, v233\n"
    "v_dot2c_f32_bf16 v16, s59, v234\n"
    "v_dot2c_f32_bf16 v17, v234, v234\n"
    "v_dot2c_f32_bf16 v16, s59, v235\n"
    "v_dot2c_f32_bf16 v17, v235, v235\n"
    "v_dot2c_f32_bf16 v16, s59, v236\n"
    "v_dot2c_f32_bf16 v17, v236, v236\n"
    "v_dot2c_f32_bf16 v16, s59, v237\n"
    "v_dot2c_f32_bf16 v17, v237, v237\n"
    "v_dot2c_f32_bf16 v16, s59, v238\n"
    "v_dot2c_f32_bf16 v17, v238, v238\n"
    "v_dot2c_f32_bf16 v16, s59, v239\n"
    "v_dot2c_f32_bf16 v17, v239, v239\n"
    "v_dot2c_f32_bf16 v20, s59, v32\n"
    "s_waitcnt lgkmcnt(14)\n"
    "v_dot2c_f32_bf16 v21, s59, v36\n"
    "v_dot2c_f32_bf16 v20, s59, v33\n"
    "v_dot2c_f32_bf16 v21, s59, v37\n"
    "v_dot2c_f32_bf16 v20, s59, v34\n"
    "v_dot2c_f32_bf16 v21, s59, v38\n"
    "v_dot2c_f32_bf16 v20, s59, v35\n"
    "v_dot2c_f32_bf16 v21, s59, v39\n"
    "v_mfma_f32_32x32x16_bf16 v[80:95], v[208:211], a[0:3], v[80:95]\n"
    "s_waitcnt vmcnt(6)\n"
    "ds_read_b128 v[240:243], v40 offset:16384\n"
    "s_waitcnt lgkmcnt(13)\n"
    "v_mfma_f32_32x32x16_bf16 v[96:111], v[208:211], a[4:7], v[96:111]\n"
    "ds_read_b128 v[244:247], v41 offset:16384\n"
    "ds_read_b128 v[248:251], v42 offset:16384\n"
    "s_add_u32 m0, s83, 65536\n"
    "s_nop 0\n"
    "buffer_load_dwordx4 v15, s[24:27], s5 offen lds\n"
    "s_add_u32 s5, s5, 172032\n"
    "s_cmp_eq_u32 s5, s99\n"
    "s_cselect_b32 s5, s98, s5\n"
    "s_waitcnt lgkmcnt(13)\n"
    "v_mfma_f32_32x32x16_bf16 v[80:95], v[212:215], a[8:11], v[80:95]\n"
    "ds_read_b128 v[252:255], v43 offset:16384\n"
    "s_waitcnt lgkmcnt(12)\n"
    "v_mfma_f32_32x32x16_bf16 v[96:111], v[212:215], a[12:15], v[96:111]\n"
    "s_waitcnt vmcnt(5)\n"
    "s_barrier\n"
    "ds_read_b64_tr_b16 v[32:33], v23 offset:40960\n"
    "ds_read_b64_tr_b16 v[34:35], v23 offset:41216\n"
    "ds_read_b64_tr_b16 v[36:37], v23 offset:41984\n"
    "ds_read_b64_tr_b16 v[38:39], v23 offset:42240\n"
    "s_add_u32 m0, s83, 69632\n"
    "s_nop 0\n"
    "buffer_load_dwordx4 v15, s[24:27], s5 offen lds\n"
    "s_add_u32 s5, s5, 172032\n"
    "s_cmp_eq_u32 s5, s99\n"
    "s_cselect_b32 s5, s98, s5\n"
    "s_waitcnt lgkmcnt(14)\n"
    "v_mfma_f32_32x32x16_bf16 v[80:95], v[216:219], a[16:19], v[80:95]\n"
    "ds_read_b64_tr_b16 a[32:33], v14 offset:40960\n"
    "ds_read_b64_tr_b16 a[34:35], v14 offset:41216\n"
    "ds_read_b64_tr_b16 a[36:37], v14 offset:41984\n"
    "ds_read_b64_tr_b16 a[38:39], v14 offset:42240\n"
    "s_waitcnt lgkmcnt(14)\n"
    "v_mfma_f32_32x32x16_bf16 v[96:111], v[216:219], a[20:23], v[96:111]\n"
    "ds_read_b64_tr_b16 a[40:41], v14 offset:43008\n"
    "ds_read_b64_tr_b16 a[42:43], v14 offset:43264\n"
    "ds_read_b64_tr_b16 a[44:45], v14 offset:44032\n"
    "ds_read_b64_tr_b16 a[46:47], v14 offset:44288\n"
    "s_add_u32 m0, s83, 73728\n"
    "s_nop 0\n"
    "buffer_load_dwordx4 v15, s[24:27], s5 offen lds\n"
    "s_add_u32 s5, s5, 172032\n"
    "s_cmp_eq_u32 s5, s99\n"
    "s_cselect_b32 s5, s98, s5\n"
    "v_mfma_f32_32x32x16_bf16 v[80:95], v[220:223], a[24:27], v[80:95]\n"
    "ds_read_b64_tr_b16 a[48:49], v14 offset:45056\n"
    "ds_read_b64_tr_b16 a[50:51], v14 offset:45312\n"
    "ds_read_b64_tr_b16 a[52:53], v14 offset:46080\n"
    "ds_read_b64_tr_b16 a[54:55], v14 offset:46336\n"
    "s_waitcnt lgkmcnt(14)\n"
    "v_mfma_f32_32x32x16_bf16 v[96:111], v[220:223], a[28:31], v[96:111]\n"
    "ds_read_b64_tr_b16 a[56:57], v14 offset:47104\n"
    "ds_read_b64_tr_b16 a[58:59], v14 offset:47360\n"
    "ds_read_b64_tr_b16 a[60:61], v14 offset:48128\n"
    "ds_read_b64_tr_b16 a[62:63], v14 offset:48384\n"
    "s_add_u32 m0, s83, 77824\n"
    "s_nop 0\n"
    "buffer_load_dwordx4 v15, s[24:27], s5 offen lds\n"
    "s_add_u32 s5, s5, 172032\n"
    "s_cmp_eq_u32 s5, s99\n"
    "s_cselect_b32 s5, s98, s5\n"
    "s_bitcmp1_b32 0x3ffc, s49\n"
    "s_cbranch_scc1 L_pace_5_%=\n"
    "s_sleep 2\n"
    "L_pace_5_%=:\n"
    "v_lshrrev_b32 v24, 3, v22\n"
    "v_mul_u32_u24 v28, 896, v24\n"
    "v_and_b32 v29, 1, v25\n"
    "v_lshlrev_b32 v30, 2, v29\n"
    "v_and_b32 v31, 7, v22\n"
    "v_xor_b32 v30, v30, v31\n"
    "v_lshl_add_u32 v4, v30, 4, v28\n"
    "s_lshr_b32 s64, s48, 1\n"
    "s_lshl_b32 s64, s64, 1\n"
    "v_mul_u32_u24 v28, 3584, v24\n"
    "v_add_u32 v29, s64, v29\n"
    "v_lshlrev_b32 v29, 1, v29\n"
    "v_xor_b32 v29, v29, v31\n"
    "v_lshl_add_u32 v6, v29, 4, v28\n"
    "s_and_b32 s64, s48, 1\n"
    "s_mul_i32 s64, s64, 28672\n"
    "s_lshl_b32 s65, s54, 7\n"
    "s_add_u32 s64, s64, s65\n"
    "v_add_u32 v6, s64, v6\n"
    "v_add_u32 v5, 57344, v6\n"
    "v_add_u32 v0, 114688, v6\n"
    "v_add_u32 v3, 172032, v6\n"
    "s_lshr_b32 s64, s48, 1\n"
    "s_mul_i32 s64, s64, 200704\n"
    "s_lshl_b32 s65, s52, 7\n"
    "s_add_u32 s64, s64, s65\n"
    "s_lshl_b32 s65, s48, 1\n"
    "s_add_u32 s65, s65, s19\n"
    "s_add_u32 s73, s65, 1\n"
    "s_and_b32 s65, s65, 3\n"
    "s_and_b32 s73, s73, 3\n"
    "s_mul_i32 s65, s65, 50176\n"
    "s_mul_i32 s73, s73, 50176\n"
    "s_add_u32 s65, s65, s64\n"
    "s_add_u32 s73, s73, s64\n"
    "s_mul_i32 s74, s48, 14336\n"
    "s_add_u32 s74, s74, 98304\n"
    "v_mul_u32_u24 v7, 896, v26\n"
    "v_lshl_add_u32 v7, v27, 4, v7\n"
    "s_lshl_b32 s75, s51, 1\n"
    "s_and_b32 s64, s48, 1\n"
    "s_add_u32 s75, s75, s64\n"
    "s_mul_i32 s75, s75, 28672\n"
    "s_lshl_b32 s64, s52, 7\n"
    "s_add_u32 s75, s75, s64\n"
    "s_lshr_b32 s64, s48, 1\n"
    "s_lshl_b32 s64, s64, 6\n"
    "s_add_u32 s75, s75, s64\n"
    "s_cmp_ge_u32 s55, 28\n"
    "s_cselect_b32 s65, 2147417856, s65\n"
    "s_cselect_b32 s73, 2147417856, s73\n"
    "s_cselect_b32 s75, 2147483392, s75\n"
    "s_add_u32 m0, s83, 81920\n"
    "s_nop 0\n"
    "buffer_load_dwordx4 v15, s[24:27], s5 offen lds\n"
    "v_dot2c_f32_bf16 v16, s59, v240\n"
    "v_dot2c_f32_bf16 v17, v240, v240\n"
    "v_dot2c_f32_bf16 v16, s59, v241\n"
    "v_dot2c_f32_bf16 v17, v241, v241\n"
    "v_dot2c_f32_bf16 v16, s59, v242\n"
    "v_dot2c_f32_bf16 v17, v242, v242\n"
    "v_dot2c_f32_bf16 v16, s59, v243\n"
    "v_dot2c_f32_bf16 v17, v243, v243\n"
    "v_dot2c_f32_bf16 v16, s59, v244\n"
    "v_dot2c_f32_bf16 v17, v244, v244\n"
    "v_dot2c_f32_bf16 v16, s59, v245\n"
    "v_dot2c_f32_bf16 v17, v245, v245\n"
    "v_dot2c_f32_bf16 v16, s59, v246\n"
    "v_dot2c_f32_bf16 v17, v246, v246\n"
    "v_dot2c_f32_bf16 v16, s59, v247\n"
    "v_dot2c_f32_bf16 v17, v247, v247\n"
    "buffer_load_dwordx4 a[72:75], v7, s[20:23], s75 offen offset:0\n"
    "v_dot2c_f32_bf16 v16, s59, v248\n"
    "v_dot2c_f32_bf16 v17, v248, v248\n"
    "v_dot2c_f32_bf16 v16, s59, v249\n"
    "v_dot2c_f32_bf16 v17, v249, v249\n"
    "v_dot2c_f32_bf16 v16, s59, v250\n"
    "v_dot2c_f32_bf16 v17, v250, v250\n"
    "v_dot2c_f32_bf16 v16, s59, v251\n"
    "v_dot2c_f32_bf16 v17, v251, v251\n"
    "v_dot2c_f32_bf16 v16, s59, v252\n"
    "v_dot2c_f32_bf16 v17, v252, v252\n"
    "v_dot2c_f32_bf16 v16, s59, v253\n"
    "v_dot2c_f32_bf16 v17, v253, v253\n"
    "v_dot2c_f32_bf16 v16, s59, v254\n"
    "v_dot2c_f32_bf16 v17, v254, v254\n"
    "v_dot2c_f32_bf16 v16, s59, v255\n"
    "v_dot2c_f32_bf16 v17, v255, v255\n"
    "s_nop 2\n"
    "v_permlane32_swap_b32 v16, v17\n"
    "v_add_f32 v16, v16, v17\n"
    "v_mov_b32 v17, v16\n"
    "s_nop 1\n"
    "v_permlane32_swap_b32 v16, v17\n"
    "v_mul_f32 v16, 0.002232142857142857, v16\n"
    "v_mul_f32 v17, 0.002232142857142857, v17\n"
    "v_fma_f32 v17, -v16, v16, v17\n"
    "v_max_f32 v17, 0, v17\n"
    "v_add_f32 v17, 1e-05, v17\n"
    "v_rsq_f32 v18, v17\n"
    "v_dot2c_f32_bf16 v20, s59, v32\n"
    "s_waitcnt lgkmcnt(14)\n"
    "v_dot2c_f32_bf16 v21, s59, v36\n"
    "v_dot2c_f32_bf16 v20, s59, v33\n"
    "v_dot2c_f32_bf16 v21, s59, v37\n"
    "v_dot2c_f32_bf16 v20, s59, v34\n"
    "v_dot2c_f32_bf16 v21, s59, v38\n"
    "v_dot2c_f32_bf16 v20, s59, v35\n"
    "v_dot2c_f32_bf16 v21, s59, v39\n"
    "v_mfma_f32_32x32x16_bf16 v[80:95], v[224:227], a[32:35], v[80:95]\n"
    "s_lshl_b32 s71, s48, 9\n"
    "s_add_u32 s71, s71, 155648\n"
    "v_mul_f32 v19, v16, v18\n"
    "s_waitcnt lgkmcnt(12)\n"
    "v_mfma_f32_32x32x16_bf16 v[96:111], v[224:227], a[36:39], v[96:111]\n"
    "s_mov_b32 s78, -1\n"
    "s_mov_b32 s79, 0\n"
    "buffer_load_dwordx4 a[76:79], v7, s[20:23], s75 offen offset:32\n"
    "s_waitcnt lgkmcnt(10)\n"
    "v_mfma_f32_32x32x16_bf16 v[80:95], v[228:231], a[40:43], v[80:95]\n"
    "v_sub_f32 v19, 0, v19\n"
    "v_lshl_add_u32 v29, v22, 2, s71\n"
    "s_waitcnt lgkmcnt(8)\n"
    "v_mfma_f32_32x32x16_bf16 v[96:111], v[228:231], a[44:47], v[96:111]\n"
    "s_waitcnt vmcnt(9)\n"
    "s_barrier\n"
    "ds_read_b64_tr_b16 v[32:33], v23 offset:49152\n"
    "ds_read_b64_tr_b16 v[34:35], v23 offset:49408\n"
    "ds_read_b64_tr_b16 v[36:37], v23 offset:50176\n"
    "ds_read_b64_tr_b16 v[38:39], v23 offset:50432\n"
    "v_cndmask_b32 v28, v19, v18, s[78:79]\n"
    "s_mul_i32 s64, s84, 56\n"
    "s_add_u32 m0, s83, 0\n"
    "s_nop 0\n"
    "buffer_load_dwordx4 v6, s[32:35], s64 offen lds\n"
    "s_waitcnt lgkmcnt(10)\n"
    "v_mfma_f32_32x32x16_bf16 v[80:95], v[232:235], a[48:51], v[80:95]\n"
    "ds_read_b64_tr_b16 a[0:1], v14 offset:49152\n"
    "ds_read_b64_tr_b16 a[2:3], v14 offset:49408\n"
    "ds_read_b64_tr_b16 a[8:9], v14 offset:51200\n"
    "ds_read_b64_tr_b16 a[10:11], v14 offset:51456\n"
    "v_lshl_add_u32 v30, v27, 4, s71\n"
    "ds_write_b32 v29, v28\n"
    "s_waitcnt lgkmcnt(13)\n"
    "v_mfma_f32_32x32x16_bf16 v[96:111], v[232:235], a[52:55], v[96:111]\n"
    "ds_read_b64_tr_b16 a[16:17], v14 offset:53248\n"
    "ds_read_b64_tr_b16 a[18:19], v14 offset:53504\n"
    "ds_read_b64_tr_b16 a[24:25], v14 offset:55296\n"
    "ds_read_b64_tr_b16 a[26:27], v14 offset:55552\n"
    "s_lshl_b32 s70, s48, 8\n"
    "s_add_u32 s70, s70, 160768\n"
    "s_add_u32 m0, s83, 4096\n"
    "s_nop 0\n"
    "buffer_load_dwordx4 v5, s[32:35], s64 offen lds\n"
    "s_waitcnt lgkmcnt(14)\n"
    "v_mfma_f32_32x32x16_bf16 v[80:95], v[236:239], a[56:59], v[80:95]\n"
    "ds_read_b64_tr_b16 a[4:5], v14 offset:50176\n"
    "ds_read_b64_tr_b16 a[6:7], v14 offset:50432\n"
    "ds_read_b64_tr_b16 a[12:13], v14 offset:52224\n"
    "ds_read_b64_tr_b16 a[14:15], v14 offset:52480\n"
    "v_lshl_add_u32 v31, v22, 2, s70\n"
    "v_lshlrev_b32 v24, 2, v26\n"
    "s_waitcnt lgkmcnt(14)\n"
    "v_mfma_f32_32x32x16_bf16 v[96:111], v[236:239], a[60:63], v[96:111]\n"
    "ds_read_b64_tr_b16 a[20:21], v14 offset:54272\n"
    "ds_read_b64_tr_b16 a[22:23], v14 offset:54528\n"
    "ds_read_b64_tr_b16 a[28:29], v14 offset:56320\n"
    "ds_read_b64_tr_b16 a[30:31], v14 offset:56576\n"
    "v_add_u32 v24, 160768, v24\n"
    "v_dot2c_f32_bf16 v20, s59, v32\n"
    "v_dot2c_f32_bf16 v21, s59, v36\n"
    "v_dot2c_f32_bf16 v20, s59, v33\n"
    "v_dot2c_f32_bf16 v21, s59, v37\n"
    "v_dot2c_f32_bf16 v20, s59, v34\n"
    "s_waitcnt lgkmcnt(14)\n"
    "v_dot2c_f32_bf16 v21, s59, v38\n"
    "v_dot2c_f32_bf16 v20, s59, v35\n"
    "v_dot2c_f32_bf16 v21, s59, v39\n"
    "s_nop 2\n"
    "v_permlane32_swap_b32 v20, v21\n"
    "v_add_f32 v20, v20, v21\n"
    "ds_write_b32 v31, v20\n"
    "v_mfma_f32_32x32x16_bf16 v[80:95], v[240:243], a[0:3], v[80:95]\n"
    "ds_read_b128 v[128:131], v30 offset:128\n"
    "ds_read_b128 v[132:135], v30 offset:160\n"
    "ds_read_b128 v[136:139], v30 offset:192\n"
    "ds_read_b128 v[140:143], v30 offset:224\n"
    "s_waitcnt lgkmcnt(14)\n"
    "v_mfma_f32_32x32x16_bf16 v[80:95], v[244:247], a[8:11], v[80:95]\n"
    "s_waitcnt lgkmcnt(0)\n"
    "s_barrier\n"
    "ds_read2_b32 v[64:65], v24 offset0:0 offset1:32\n"
    "ds_read2_b32 v[66:67], v24 offset0:64 offset1:96\n"
    "ds_read2_b32 v[68:69], v24 offset0:128 offset1:160\n"
    "ds_read2_b32 v[70:71], v24 offset0:192 offset1:224\n"
    "v_mfma_f32_32x32x16_bf16 v[80:95], v[248:251], a[16:19], v[80:95]\n"
    "ds_read_b128 v[112:115], v30 offset:0\n"
    "ds_read_b128 v[116:119], v30 offset:32\n"
    "s_add_u32 m0, s83, 8192\n"
    "s_nop 0\n"
    "buffer_load_dwordx4 v0, s[32:35], s64 offen lds\n"
    "v_mfma_f32_32x32x16_bf16 v[80:95], v[252:255], a[24:27], v[80:95]\n"
    "ds_read_b128 v[120:123], v30 offset:64\n"
    "ds_read_b128 v[124:127], v30 offset:96\n"
    "s_waitcnt lgkmcnt(6)\n"
    "v_add_f32 v64, v64, v66\n"
    "v_add_f32 v65, v65, v67\n"
    "s_waitcnt lgkmcnt(4)\n"
    "v_add_f32 v68, v68, v70\n"
    "v_add_f32 v69, v69, v71\n"
    "v_mfma_f32_32x32x16_bf16 v[96:111], v[240:243], a[4:7], v[96:111]\n"
    "v_add_f32 v20, v64, v68\n"
    "v_add_f32 v21, v65, v69\n"
    "s_mov_b32 s76, 0x10000000\n"
    "s_mov_b32 s77, 0x10000000\n"
    "s_add_u32 m0, s83, 12288\n"
    "s_nop 0\n"
    "buffer_load_dwordx4 v3, s[32:35], s64 offen lds\n"
    "v_mfma_f32_32x32x16_bf16 v[96:111], v[244:247], a[12:15], v[96:111]\n"
    "v_fma_f32 v48, v128, v20, s80\n"
    "v_fma_f32 v49, v129, v20, s80\n"
    "v_fma_f32 v50, v130, v20, s80\n"
    "v_fma_f32 v51, v131, v20, s80\n"
    "v_fma_f32 v52, v132, v20, s80\n"
    "v_fma_f32 v53, v133, v20, s80\n"
    "v_mfma_f32_32x32x16_bf16 v[96:111], v[248:251], a[20:23], v[96:111]\n"
    "v_fma_f32 v54, v134, v20, s80\n"
    "v_fma_f32 v55, v135, v20, s80\n"
    "v_fma_f32 v56, v136, v20, s80\n"
    "v_fma_f32 v57, v137, v20, s80\n"
    "v_fma_f32 v58, v138, v20, s80\n"
    "v_mfma_f32_32x32x16_bf16 v[96:111], v[252:255], a[28:31], v[96:111]\n"
    "v_fma_f32 v59, v139, v20, s80\n"
    "v_fma_f32 v60, v140, v20, s80\n"
    "v_fma_f32 v61, v141, v20, s80\n"
    "v_fma_f32 v62, v142, v20, s80\n"
    "v_fma_f32 v63, v143, v20, s80\n"
    "s_add_u32 s56, s56, 1\n"
    "s_and_b32 s58, s56, 0xfff\n"
    "s_waitcnt lgkmcnt(3)\n"
    "v_pk_fma_f32 v[48:49], v[80:81], v[112:113], v[48:49]\n"
    "v_pk_fma_f32 v[50:51], v[82:83], v[114:115], v[50:51]\n"
    "s_mul_i32 s64, s85, 56\n"
    "s_add_u32 m0, s83, 16384\n"
    "s_nop 0\n"
    "buffer_load_dwordx4 v6, s[32:35], s64 offen lds\n"
    "s_waitcnt lgkmcnt(2)\n"
    "v_pk_fma_f32 v[52:53], v[84:85], v[116:117], v[52:53]\n"
    "v_pk_fma_f32 v[54:55], v[86:87], v[118:119], v[54:55]\n"
    "s_waitcnt lgkmcnt(1)\n"
    "v_pk_fma_f32 v[56:57], v[88:89], v[120:121], v[56:57]\n"
    "v_pk_fma_f32 v[58:59], v[90:91], v[122:123], v[58:59]\n"
    "s_waitcnt lgkmcnt(0)\n"
    "v_pk_fma_f32 v[60:61], v[92:93], v[124:125], v[60:61]\n"
    "v_pk_fma_f32 v[62:63], v[94:95], v[126:127], v[62:63]\n"
    "v_pk_mul_f32 v[64:65], v[48:49], s[80:81] op_sel:[0,1] op_sel_hi:[1,1]\n"
    "v_pk_mul_f32 v[66:67], v[50:51], s[80:81] op_sel:[0,1] op_sel_hi:[1,1]\n"
    "v_pk_mul_f32 v[68:69], v[52:53], s[80:81] op_sel:[0,1] op_sel_hi:[1,1]\n"
    "v_pk_mul_f32 v[70:71], v[54:55], s[80:81] op_sel:[0,1] op_sel_hi:[1,1]\n"
    "v_pk_mul_f32 v[72:73], v[56:57], s[80:81] op_sel:[0,1] op_sel_hi:[1,1]\n"
    "v_pk_mul_f32 v[74:75], v[58:59], s[80:81] op_sel:[0,1] op_sel_hi:[1,1]\n"
    "v_pk_mul_f32 v[76:77], v[60:61], s[80:81] op_sel:[0,1] op_sel_hi:[1,1]\n"
    "v_pk_mul_f32 v[78:79], v[62:63], s[80:81] op_sel:[0,1] op_sel_hi:[1,1]\n"
    "s_add_u32 m0, s83, 20480\n"
    "s_nop 0\n"
    "buffer_load_dwordx4 v5, s[32:35], s64 offen lds\n"
    "v_exp_f32_e64 v64, v64 clamp\n"
    "v_max_f32 v48, s80, v48\n"
    "v_exp_f32_e64 v65, v65 clamp\n"
    "v_max_f32 v49, s80, v49\n"
    "v_exp_f32_e64 v66, v66 clamp\n"
    "v_max_f32 v50, s80, v50\n"
    "v_exp_f32_e64 v67, v67 clamp\n"
    "v_max_f32 v51, s80, v51\n"
    "v_exp_f32_e64 v68, v68 clamp\n"
    "v_max_f32 v52, s80, v52\n"
    "v_exp_f32_e64 v69, v69 clamp\n"
    "v_max_f32 v53, s80, v53\n"
    "v_exp_f32_e64 v70, v70 clamp\n"
    "v_max_f32 v54, s80, v54\n"
    "v_exp_f32_e64 v71, v71 clamp\n"
    "v_max_f32 v55, s80, v55\n"
    "v_exp_f32_e64 v72, v72 clamp\n"
    "v_max_f32 v56, s80, v56\n"
    "v_exp_f32_e64 v73, v73 clamp\n"
    "v_max_f32 v57, s80, v57\n"
    "v_exp_f32_e64 v74, v74 clamp\n"
    "v_max_f32 v58, s80, v58\n"
    "v_exp_f32_e64 v75, v75 clamp\n"
    "v_max_f32 v59, s80, v59\n"
    "v_exp_f32_e64 v76, v76 clamp\n"
    "v_max_f32 v60, s80, v60\n"
    "v_exp_f32_e64 v77, v77 clamp\n"
    "v_max_f32 v61, s80, v61\n"
    "s_add_u32 m0, s83, 24576\n"
    "s_nop 0\n"
    "buffer_load_dwordx4 v0, s[32:35], s64 offen lds\n"
    "v_exp_f32_e64 v78, v78 clamp\n"
    "v_max_f32 v62, s80, v62\n"
    "v_exp_f32_e64 v79, v79 clamp\n"
    "v_max_f32 v63, s80, v63\n"
    "v_pk_add_f32 v[48:49], v[48:49], v[64:65]\n"
    "v_pk_add_f32 v[50:51], v[50:51], v[66:67]\n"
    "v_pk_add_f32 v[52:53], v[52:53], v[68:69]\n"
    "v_pk_add_f32 v[54:55], v[54:55], v[70:71]\n"
    "v_pk_add_f32 v[56:57], v[56:57], v[72:73]\n"
    "v_pk_add_f32 v[58:59], v[58:59], v[74:75]\n"
    "v_pk_add_f32 v[60:61], v[60:61], v[76:77]\n"
    "v_pk_add_f32 v[62:63], v[62:63], v[78:79]\n"
    "v_cvt_pk_bf16_f32 v32, v48, v49\n"
    "v_cvt_pk_bf16_f32 v33, v50, v51\n"
    "v_cvt_pk_bf16_f32 v34, v52, v53\n"
    "v_cvt_pk_bf16_f32 v35, v54, v55\n"
    "s_add_u32 m0, s83, 28672\n"
    "s_nop 0\n"
    "buffer_load_dwordx4 v3, s[32:35], s64 offen lds\n"
    "v_cvt_pk_bf16_f32 v36, v56, v57\n"
    "v_cvt_pk_bf16_f32 v37, v58, v59\n"
    "v_cvt_pk_bf16_f32 v38, v60, v61\n"
    "v_cvt_pk_bf16_f32 v39, v62, v63\n"
    "v_pk_mul_f32 v[128:129], v[128:129], v[20:21] op_sel:[0,1] op_sel_hi:[1,1]\n"
    "v_pk_mul_f32 v[130:131], v[130:131], v[20:21] op_sel:[0,1] op_sel_hi:[1,1]\n"
    "v_pk_mul_f32 v[132:133], v[132:133], v[20:21] op_sel:[0,1] op_sel_hi:[1,1]\n"
    "v_pk_mul_f32 v[134:135], v[134:135], v[20:21] op_sel:[0,1] op_sel_hi:[1,1]\n"
    "v_pk_mul_f32 v[136:137], v[136:137], v[20:21] op_sel:[0,1] op_sel_hi:[1,1]\n"
    "v_pk_mul_f32 v[138:139], v[138:139], v[20:21] op_sel:[0,1] op_sel_hi:[1,1]\n"
    "v_pk_mul_f32 v[140:141], v[140:141], v[20:21] op_sel:[0,1] op_sel_hi:[1,1]\n"
    "v_pk_mul_f32 v[142:143], v[142:143], v[20:21] op_sel:[0,1] op_sel_hi:[1,1]\n"
    "v_pk_fma_f32 v[96:97], v[96:97], v[112:113], v[128:129]\n"
    "v_pk_fma_f32 v[98:99], v[98:99], v[114:115], v[130:131]\n"
    "s_add_u32 m0, s74, 0\n"
    "s_nop 0\n"
    "buffer_load_dwordx4 v4, s[28:31], s65 offen lds\n"
    "s_add_u32 s65, s65, 7168\n"
    "v_pk_fma_f32 v[100:101], v[100:101], v[116:117], v[132:133]\n"
    "v_pk_fma_f32 v[102:103], v[102:103], v[118:119], v[134:135]\n"
    "v_pk_fma_f32 v[104:105], v[104:105], v[120:121], v[136:137]\n"
    "v_pk_fma_f32 v[106:107], v[106:107], v[122:123], v[138:139]\n"
    "v_pk_fma_f32 v[108:109], v[108:109], v[124:125], v[140:141]\n"
    "v_pk_fma_f32 v[110:111], v[110:111], v[126:127], v[142:143]\n"
    "v_mov_b32 v28, s59\n"
    "v_cvt_pk_bf16_f32 v40, v96, v97\n"
    "v_cvt_pk_bf16_f32 v41, v98, v99\n"
    "v_cvt_pk_bf16_f32 v42, v100, v101\n"
    "v_cvt_pk_bf16_f32 v43, v102, v103\n"
    "v_cvt_pk_bf16_f32 v44, v104, v105\n"
    "v_cvt_pk_bf16_f32 v45, v106, v107\n"
    "v_cvt_pk_bf16_f32 v46, v108, v109\n"
    "s_add_u32 m0, s74, 1024\n"
    "s_nop 0\n"
    "buffer_load_dwordx4 v4, s[28:31], s65 offen lds\n"
    "s_add_u32 s65, s65, 7168\n"
    "v_cvt_pk_bf16_f32 v47, v110, v111\n"
    "v_cndmask_b32 v40, v40, v28, s[76:77]\n"
    "v_cndmask_b32 v41, v41, v28, s[76:77]\n"
    "v_cndmask_b32 v42, v42, v28, s[76:77]\n"
    "v_cndmask_b32 v43, v43, v28, s[76:77]\n"
    "v_cndmask_b32 v44, v44, v28, s[76:77]\n"
    "v_cndmask_b32 v45, v45, v28, s[76:77]\n"
    "v_cndmask_b32 v46, v46, v28, s[76:77]\n"
    "v_cndmask_b32 v47, v47, v28, s[76:77]\n"
    "v_mfma_f32_32x32x16_bf16 v[80:95], v[32:35], v[40:43], 0\n"
    "s_nop 0\n"
    "v_mfma_f32_32x32x16_bf16 v[80:95], v[36:39], v[44:47], v[80:95]\n"
    "v_add_u32 v28, s91, v1\n"
    "v_add_u32 v31, s83, v1\n"
    "v_mov_b32 v24, 0xfffff000\n"
    "s_lshl_b32 s60, s49, 6\n"
    "s_lshl_b32 s61, s48, 4\n"
    "s_add_u32 s60, s60, s61\n"
    "s_lshl_b32 s60, s60, 10\n"
    "s_add_u32 s60, s60, 1048576\n"
    "s_lshl_b32 s66, s50, 10\n"
    "s_add_u32 s66, s66, s60\n"
    "s_xor_b32 s60, s60, 0x10000\n"
    "s_xor_b32 s49, s49, 1\n"
    "s_and_b32 s67, s49, 1\n"
    "s_lshl_b32 s67, s67, 4\n"
    "s_add_u32 s67, s67, 57336\n"
    "s_add_u32 s70, s50, 3\n"
    "s_and_b32 s70, s70, 3\n"
    "s_lshl_b32 s70, s70, 12\n"
    "s_add_u32 s63, s60, s70\n"
    "s_add_u32 s70, s50, 2\n"
    "s_and_b32 s70, s70, 3\n"
    "s_lshl_b32 s70, s70, 12\n"
    "s_add_u32 s62, s60, s70\n"
    "s_add_u32 s70, s50, 1\n"
    "s_and_b32 s70, s70, 3\n"
    "s_lshl_b32 s70, s70, 12\n"
    "s_add_u32 s61, s60, s70\n"
    "s_add_u32 s70, s50, 0\n"
    "s_and_b32 s70, s70, 3\n"
    "s_lshl_b32 s70, s70, 12\n"
    "s_add_u32 s60, s60, s70\n"
    "ds_write_b128 v28, v[80:83] offset:40960\n"
    "ds_write_b128 v28, v[84:87] offset:41984\n"
    "ds_write_b128 v28, v[88:91] offset:43008\n"
    "ds_write_b128 v28, v[92:95] offset:44032\n"
    "s_waitcnt lgkmcnt(0)\n"
    "s_barrier\n"
    "ds_read_b128 v[96:99], v31 offset:40960\n"
    "ds_read_b128 v[100:103], v31 offset:45056\n"
    "ds_read_b128 v[104:107], v31 offset:49152\n"
    "ds_read_b128 v[108:111], v31 offset:53248\n"
    "v_add_u32 v8, s67, v14\n"
    "v_add_u32 v9, s83, v8\n"
    "v_mov_b32 v20, 0\n"
    "v_mov_b32 v21, 0\n"
    "s_waitcnt lgkmcnt(2)\n"
    "v_pk_add_f32 v[96:97], v[96:97], v[100:101]\n"
    "v_pk_add_f32 v[98:99], v[98:99], v[102:103]\n"
    "s_waitcnt lgkmcnt(0)\n"
    "v_pk_add_f32 v[104:105], v[104:105], v[108:109]\n"
    "v_pk_add_f32 v[106:107], v[106:107], v[110:111]\n"
    "v_pk_add_f32 v[96:97], v[96:97], v[104:105]\n"
    "v_pk_add_f32 v[98:99], v[98:99], v[106:107]\n"
    "v_and_or_b32 v96, v96, v24, s58\n"
    "buffer_store_dwordx4 v[96:99], v1, s[44:47], s66 offen sc1\n"
    "s_waitcnt vmcnt(13)\n"
    "ds_read_b64_tr_b16 v[32:33], v9 offset:0\n"
    "ds_read_b64_tr_b16 v[34:35], v9 offset:256\n"
    "ds_read_b64_tr_b16 v[36:37], v9 offset:4096\n"
    "ds_read_b64_tr_b16 v[38:39], v9 offset:4352\n"
    "ds_read_b64_tr_b16 v[40:41], v9 offset:8192\n"
    "ds_read_b64_tr_b16 v[42:43], v9 offset:8448\n"
    "ds_read_b64_tr_b16 v[44:45], v9 offset:12288\n"
    "ds_read_b64_tr_b16 v[46:47], v9 offset:12544\n"
    "ds_read_b64_tr_b16 v[48:49], v9 offset:16384\n"
    "ds_read_b64_tr_b16 v[50:51], v9 offset:16640\n"
    "ds_read_b64_tr_b16 v[52:53], v9 offset:20480\n"
    "ds_read_b64_tr_b16 v[54:55], v9 offset:20736\n"
    "ds_read_b64_tr_b16 v[56:57], v9 offset:24576\n"
    "ds_read_b64_tr_b16 v[58:59], v9 offset:24832\n"
    "s_barrier\n"
    "ds_read_b64_tr_b16 a[224:225], v8 offset:0\n"
    "ds_read_b64_tr_b16 a[226:227], v8 offset:256\n"
    "ds_read_b64_tr_b16 a[228:229], v8 offset:1024\n"
    "ds_read_b64_tr_b16 a[230:231], v8 offset:1280\n"
    "ds_read_b64_tr_b16 a[232:233], v8 offset:2048\n"
    "ds_read_b64_tr_b16 a[234:235], v8 offset:2304\n"
    "ds_read_b64_tr_b16 a[236:237], v8 offset:3072\n"
    "ds_read_b64_tr_b16 a[238:239], v8 offset:3328\n"
    "ds_read_b64_tr_b16 a[240:241], v8 offset:4096\n"
    "ds_read_b64_tr_b16 a[242:243], v8 offset:4352\n"
    "ds_read_b64_tr_b16 a[244:245], v8 offset:5120\n"
    "ds_read_b64_tr_b16 a[246:247], v8 offset:5376\n"
    "s_add_u32 m0, s74, 2048\n"
    "s_nop 0\n"
    "buffer_load_dwordx4 v4, s[28:31], s65 offen lds\n"
    "s_add_u32 s65, s65, 7168\n"
    "s_waitcnt lgkmcnt(14)\n"
    "v_dot2c_f32_bf16 v20, s59, v32\n"
    "v_dot2c_f32_bf16 v21, s59, v33\n"
    "v_dot2c_f32_bf16 v20, s59, v34\n"
    "v_dot2c_f32_bf16 v21, s59, v35\n"
    "v_dot2c_f32_bf16 v20, s59, v36\n"
    "v_dot2c_f32_bf16 v21, s59, v37\n"
    "v_dot2c_f32_bf16 v20, s59, v38\n"
    "v_dot2c_f32_bf16 v21, s59, v39\n"
    "v_dot2c_f32_bf16 v20, s59, v40\n"
    "v_dot2c_f32_bf16 v21, s59, v41\n"
    "v_dot2c_f32_bf16 v20, s59, v42\n"
    "v_dot2c_f32_bf16 v21, s59, v43\n"
    "v_dot2c_f32_bf16 v20, s59, v44\n"
    "v_dot2c_f32_bf16 v21, s59, v45\n"
    "s_add_u32 m0, s74, 3072\n"
    "s_nop 0\n"
    "buffer_load_dwordx4 v4, s[28:31], s65 offen lds\n"
    "s_add_u32 s65, s65, 7168\n"
    "v_dot2c_f32_bf16 v20, s59, v46\n"
    "v_dot2c_f32_bf16 v21, s59, v47\n"
    "v_dot2c_f32_bf16 v20, s59, v48\n"
    "v_dot2c_f32_bf16 v21, s59, v49\n"
    "v_dot2c_f32_bf16 v20, s59, v50\n"
    "v_dot2c_f32_bf16 v21, s59, v51\n"
    "v_dot2c_f32_bf16 v20, s59, v52\n"
    "v_dot2c_f32_bf16 v21, s59, v53\n"
    "v_dot2c_f32_bf16 v20, s59, v54\n"
    "v_dot2c_f32_bf16 v21, s59, v55\n"
    "s_waitcnt lgkmcnt(13)\n"
    "v_dot2c_f32_bf16 v20, s59, v56\n"
    "v_dot2c_f32_bf16 v21, s59, v57\n"
    "s_waitcnt lgkmcnt(12)\n"
    "v_dot2c_f32_bf16 v20, s59, v58\n"
    "v_dot2c_f32_bf16 v21, s59, v59\n"
    "s_lshl_b32 s70, s48, 8\n"
    "s_add_u32 s70, s70, 161792\n"
    "v_lshl_add_u32 v31, v22, 2, s70\n"
    "v_add_f32 v20, v20, v21\n"
    "ds_write_b32 v31, v20\n"
    "s_waitcnt lgkmcnt(11)\n"
    "v_mfma_f32_32x32x16_bf16 v[32:47], a[224:227], v[144:147], 0\n"
    "ds_read_b64_tr_b16 a[248:249], v8 offset:6144\n"
    "ds_read_b64_tr_b16 a[250:251], v8 offset:6400\n"
    "v_lshlrev_b32 v24, 2, v26\n"
    "v_add_u32 v24, 161792, v24\n"
    "s_add_u32 m0, s74, 4096\n"
    "s_nop 0\n"
    "buffer_load_dwordx4 v4, s[28:31], s65 offen lds\n"
    "s_add_u32 s65, s65, 7168\n"
    "s_waitcnt lgkmcnt(11)\n"
    "v_mfma_f32_32x32x16_bf16 v[32:47], a[228:231], v[148:151], v[32:47]\n"
    "ds_read_b64_tr_b16 a[252:253], v8 offset:7168\n"
    "ds_read_b64_tr_b16 a[254:255], v8 offset:7424\n"
    "s_add_u32 m0, s74, 5120\n"
    "s_nop 0\n"
    "buffer_load_dwordx4 v4, s[28:31], s65 offen lds\n"
    "s_add_u32 s65, s65, 7168\n"
    "s_waitcnt lgkmcnt(11)\n"
    "v_mfma_f32_32x32x16_bf16 v[32:47], a[232:235], v[152:155], v[32:47]\n"
    "ds_read_b64_tr_b16 a[224:225], v8 offset:8192\n"
    "ds_read_b64_tr_b16 a[226:227], v8 offset:8448\n"
    "s_add_u32 m0, s74, 6144\n"
    "s_nop 0\n"
    "buffer_load_dwordx4 v4, s[28:31], s65 offen lds\n"
    "s_waitcnt lgkmcnt(11)\n"
    "v_mfma_f32_32x32x16_bf16 v[32:47], a[236:239], v[156:159], v[32:47]\n"
    "ds_read_b64_tr_b16 a[228:229], v8 offset:9216\n"
    "ds_read_b64_tr_b16 a[230:231], v8 offset:9472\n"
    "s_waitcnt lgkmcnt(8)\n"
    "s_barrier\n"
    "ds_read2_b32 v[64:65], v24 offset0:0 offset1:32\n"
    "ds_read2_b32 v[66:67], v24 offset0:64 offset1:96\n"
    "ds_read2_b32 v[68:69], v24 offset0:128 offset1:160\n"
    "ds_read2_b32 v[70:71], v24 offset0:192 offset1:224\n"
    "s_add_u32 m0, s74, 7168\n"
    "s_nop 0\n"
    "buffer_load_dwordx4 v4, s[28:31], s73 offen lds\n"
    "s_add_u32 s73, s73, 7168\n"
    "v_mfma_f32_32x32x16_bf16 v[32:47], a[240:243], v[160:163], v[32:47]\n"
    "ds_read_b64_tr_b16 a[232:233], v8 offset:10240\n"
    "ds_read_b64_tr_b16 a[234:235], v8 offset:10496\n"
    "s_add_u32 m0, s74, 8192\n"
    "s_nop 0\n"
    "buffer_load_dwordx4 v4, s[28:31], s73 offen lds\n"
    "s_add_u32 s73, s73, 7168\n"
    "v_mfma_f32_32x32x16_bf16 v[32:47], a[244:247], v[164:167], v[32:47]\n"
    "ds_read_b64_tr_b16 a[236:237], v8 offset:11264\n"
    "ds_read_b64_tr_b16 a[238:239], v8 offset:11520\n"
    "s_waitcnt lgkmcnt(7)\n"
    "v_add_f32 v64, v64, v65\n"
    "s_waitcnt lgkmcnt(6)\n"
    "v_add_f32 v66, v66, v67\n"
    "s_waitcnt lgkmcnt(5)\n"
    "v_add_f32 v68, v68, v69\n"
    "s_add_u32 m0, s74, 9216\n"
    "s_nop 0\n"
    "buffer_load_dwordx4 v4, s[28:31], s73 offen lds\n"
    "s_add_u32 s73, s73, 7168\n"
    "v_mfma_f32_32x32x16_bf16 v[32:47], a[248:251], v[168:171], v[32:47]\n"
    "ds_read_b64_tr_b16 a[240:241], v8 offset:12288\n"
    "ds_read_b64_tr_b16 a[242:243], v8 offset:12544\n"
    "s_waitcnt lgkmcnt(6)\n"
    "v_add_f32 v70, v70, v71\n"
    "v_add_f32 v64, v64, v66\n"
    "v_add_f32 v68, v68, v70\n"
    "s_add_u32 m0, s74, 10240\n"
    "s_nop 0\n"
    "buffer_load_dwordx4 v4, s[28:31], s73 offen lds\n"
    "s_add_u32 s73, s73, 7168\n"
    "v_mfma_f32_32x32x16_bf16 v[32:47], a[252:255], v[172:175], v[32:47]\n"
    "ds_read_b64_tr_b16 a[244:245], v8 offset:13312\n"
    "ds_read_b64_tr_b16 a[246:247], v8 offset:13568\n"
    "v_add_f32 v64, v64, v68\n"
    "ds_write_b32 v29, v64 offset:256\n"
    "s_add_u32 m0, s74, 11264\n"
    "s_nop 0\n"
    "buffer_load_dwordx4 v4, s[28:31], s73 offen lds\n"
    "s_add_u32 s73, s73, 7168\n"
    "v_mfma_f32_32x32x16_bf16 v[32:47], a[224:227], v[176:179], v[32:47]\n"
    "ds_read_b64_tr_b16 a[248:249], v8 offset:14336\n"
    "ds_read_b64_tr_b16 a[250:251], v8 offset:14592\n"
    "ds_read_b128 v[48:51], v30 offset:256\n"
    "v_lshrrev_b32 v24, 3, v26\n"
    "v_and_b32 v28, 3, v26\n"
    "v_lshl_add_u32 v24, v24, 2, v28\n"
    "s_add_u32 m0, s74, 12288\n"
    "s_nop 0\n"
    "buffer_load_dwordx4 v4, s[28:31], s73 offen lds\n"
    "s_add_u32 s73, s73, 7168\n"
    "v_mfma_f32_32x32x16_bf16 v[32:47], a[228:231], v[180:183], v[32:47]\n"
    "ds_read_b64_tr_b16 a[252:253], v8 offset:15360\n"
    "ds_read_b64_tr_b16 a[254:255], v8 offset:15616\n"
    "ds_read_b128 v[52:55], v30 offset:288\n"
    "v_bfe_u32 v28, v26, 2, 1\n"
    "v_mad_u32_u24 v28, v28, 14, v24\n"
    "s_add_u32 m0, s74, 13312\n"
    "s_nop 0\n"
    "buffer_load_dwordx4 v4, s[28:31], s73 offen lds\n"
    "s_waitcnt lgkmcnt(13)\n"
    "v_mfma_f32_32x32x16_bf16 v[32:47], a[232:235], v[184:187], v[32:47]\n"
    "ds_read_b64_tr_b16 a[224:225], v8 offset:16384\n"
    "ds_read_b64_tr_b16 a[226:227], v8 offset:16640\n"
    "ds_read_b128 v[56:59], v30 offset:320\n"
    "v_cmp_gt_u32 vcc, 14, v24\n"
    "s_nop 1\n"
    "v_cndmask_b32 v28, 28, v28, vcc\n"
    "s_mul_i32 s64, s86, 56\n"
    "s_add_u32 m0, s83, 32768\n"
    "s_nop 0\n"
    "buffer_load_dwordx4 v6, s[32:35], s64 offen lds\n"
    "s_waitcnt lgkmcnt(14)\n"
    "v_mfma_f32_32x32x16_bf16 v[32:47], a[236:239], v[188:191], v[32:47]\n"
    "ds_read_b64_tr_b16 a[228:229], v8 offset:17408\n"
    "ds_read_b64_tr_b16 a[230:231], v8 offset:17664\n"
    "ds_read_b128 v[60:63], v30 offset:352\n"
    "v_lshl_add_u32 v28, v27, 5, v28\n"
    "v_lshlrev_b32 v10, 4, v28\n"
    "s_add_u32 m0, s83, 36864\n"
    "s_nop 0\n"
    "buffer_load_dwordx4 v5, s[32:35], s64 offen lds\n"
    "s_waitcnt lgkmcnt(14)\n"
    "v_mfma_f32_32x32x16_bf16 v[32:47], a[240:243], v[192:195], v[32:47]\n"
    "ds_read_b64_tr_b16 a[232:233], v8 offset:18432\n"
    "ds_read_b64_tr_b16 a[234:235], v8 offset:18688\n"
    "s_mul_i32 s64, s89, 56\n"
    "s_add_u32 m0, s83, 86016\n"
    "s_nop 0\n"
    "buffer_load_dwordx4 v5, s[32:35], s64 offen lds\n"
    "s_waitcnt lgkmcnt(14)\n"
    "v_mfma_f32_32x32x16_bf16 v[32:47], a[244:247], v[196:199], v[32:47]\n"
    "ds_read_b64_tr_b16 a[236:237], v8 offset:19456\n"
    "ds_read_b64_tr_b16 a[238:239], v8 offset:19712\n"
    "s_add_u32 m0, s83, 90112\n"
    "s_nop 0\n"
    "buffer_load_dwordx4 v0, s[32:35], s64 offen lds\n"
    "s_waitcnt lgkmcnt(14)\n"
    "v_mfma_f32_32x32x16_bf16 v[32:47], a[248:251], v[200:203], v[32:47]\n"
    "ds_read_b64_tr_b16 a[240:241], v8 offset:20480\n"
    "ds_read_b64_tr_b16 a[242:243], v8 offset:20736\n"
    "s_waitcnt lgkmcnt(14)\n"
    "v_fma_f32 v48, v48, v19, s80\n"
    "v_fma_f32 v49, v49, v19, s80\n"
    "buffer_load_dwordx4 v[80:83], v10, s[44:47], s60 offen offset:0 sc1\n"
    "s_waitcnt lgkmcnt(13)\n"
    "v_mfma_f32_32x32x16_bf16 v[32:47], a[252:255], v[204:207], v[32:47]\n"
    "ds_read_b64_tr_b16 a[244:245], v8 offset:21504\n"
    "ds_read_b64_tr_b16 a[246:247], v8 offset:21760\n"
    "v_fma_f32 v50, v50, v19, s80\n"
    "v_fma_f32 v51, v51, v19, s80\n"
    "s_waitcnt lgkmcnt(12)\n"
    "v_mfma_f32_32x32x16_bf16 v[32:47], a[224:227], v[208:211], v[32:47]\n"
    "ds_read_b64_tr_b16 a[248:249], v8 offset:22528\n"
    "ds_read_b64_tr_b16 a[250:251], v8 offset:22784\n"
    "v_fma_f32 v52, v52, v19, s80\n"
    "v_fma_f32 v53, v53, v19, s80\n"
    "buffer_load_dwordx4 v[84:87], v10, s[44:47], s60 offen offset:1024 sc1\n"
    "s_waitcnt lgkmcnt(11)\n"
    "v_mfma_f32_32x32x16_bf16 v[32:47], a[228:231], v[212:215], v[32:47]\n"
    "ds_read_b64_tr_b16 a[252:253], v8 offset:23552\n"
    "ds_read_b64_tr_b16 a[254:255], v8 offset:23808\n"
    "v_fma_f32 v54, v54, v19, s80\n"
    "v_fma_f32 v55, v55, v19, s80\n"
    "s_waitcnt lgkmcnt(10)\n"
    "v_mfma_f32_32x32x16_bf16 v[32:47], a[232:235], v[216:219], v[32:47]\n"
    "ds_read_b64_tr_b16 a[224:225], v8 offset:24576\n"
    "ds_read_b64_tr_b16 a[226:227], v8 offset:24832\n"
    "v_fma_f32 v56, v56, v19, s80\n"
    "v_fma_f32 v57, v57, v19, s80\n"
    "buffer_load_dwordx4 v[88:91], v10, s[44:47], s60 offen offset:2048 sc1\n"
    "s_waitcnt lgkmcnt(10)\n"
    "v_mfma_f32_32x32x16_bf16 v[32:47], a[236:239], v[220:223], v[32:47]\n"
    "ds_read_b64_tr_b16 a[228:229], v8 offset:25600\n"
    "ds_read_b64_tr_b16 a[230:231], v8 offset:25856\n"
    "v_fma_f32 v58, v58, v19, s80\n"
    "v_fma_f32 v59, v59, v19, s80\n"
    "s_waitcnt lgkmcnt(10)\n"
    "v_mfma_f32_32x32x16_bf16 v[32:47], a[240:243], v[224:227], v[32:47]\n"
    "ds_read_b64_tr_b16 a[232:233], v8 offset:26624\n"
    "ds_read_b64_tr_b16 a[234:235], v8 offset:26880\n"
    "v_fma_f32 v60, v60, v19, s80\n"
    "v_fma_f32 v61, v61, v19, s80\n"
    "buffer_load_dwordx4 v[92:95], v10, s[44:47], s60 offen offset:3072 sc1\n"
    "s_waitcnt lgkmcnt(10)\n"
    "v_mfma_f32_32x32x16_bf16 v[32:47], a[244:247], v[228:231], v[32:47]\n"
    "ds_read_b64_tr_b16 a[236:237], v8 offset:27648\n"
    "ds_read_b64_tr_b16 a[238:239], v8 offset:27904\n"
    "v_fma_f32 v62, v62, v19, s80\n"
    "v_fma_f32 v63, v63, v19, s80\n"
    "s_waitcnt lgkmcnt(10)\n"
    "v_mfma_f32_32x32x16_bf16 v[32:47], a[248:251], v[232:235], v[32:47]\n"
    "buffer_load_dwordx4 v[96:99], v10, s[44:47], s61 offen offset:0 sc1\n"
    "s_waitcnt lgkmcnt(8)\n"
    "v_mfma_f32_32x32x16_bf16 v[32:47], a[252:255], v[236:239], v[32:47]\n"
    "s_waitcnt lgkmcnt(6)\n"
    "v_mfma_f32_32x32x16_bf16 v[32:47], a[224:227], v[240:243], v[32:47]\n"
    "buffer_load_dwordx4 v[100:103], v10, s[44:47], s61 offen offset:1024 sc1\n"
    "s_waitcnt lgkmcnt(4)\n"
    "v_mfma_f32_32x32x16_bf16 v[32:47], a[228:231], v[244:247], v[32:47]\n"
    "s_waitcnt lgkmcnt(2)\n"
    "v_mfma_f32_32x32x16_bf16 v[32:47], a[232:235], v[248:251], v[32:47]\n"
    "buffer_load_dwordx4 v[104:107], v10, s[44:47], s61 offen offset:2048 sc1\n"
    "s_waitcnt lgkmcnt(0)\n"
    "v_mfma_f32_32x32x16_bf16 v[32:47], a[236:239], v[252:255], v[32:47]\n"
    "buffer_load_dwordx4 v[108:111], v10, s[44:47], s61 offen offset:3072 sc1\n"
    "s_nop 10\n"
    "v_pk_fma_f32 v[48:49], v[32:33], v[18:19], v[48:49] op_sel_hi:[1,0,1]\n"
    "v_pk_fma_f32 v[50:51], v[34:35], v[18:19], v[50:51] op_sel_hi:[1,0,1]\n"
    "v_pk_fma_f32 v[52:53], v[36:37], v[18:19], v[52:53] op_sel_hi:[1,0,1]\n"
    "buffer_load_dwordx4 v[112:115], v10, s[44:47], s62 offen offset:0 sc1\n"
    "v_pk_fma_f32 v[54:55], v[38:39], v[18:19], v[54:55] op_sel_hi:[1,0,1]\n"
    "v_pk_fma_f32 v[56:57], v[40:41], v[18:19], v[56:57] op_sel_hi:[1,0,1]\n"
    "v_pk_fma_f32 v[58:59], v[42:43], v[18:19], v[58:59] op_sel_hi:[1,0,1]\n"
    "buffer_load_dwordx4 v[116:119], v10, s[44:47], s62 offen offset:1024 sc1\n"
    "v_pk_fma_f32 v[60:61], v[44:45], v[18:19], v[60:61] op_sel_hi:[1,0,1]\n"
    "v_pk_fma_f32 v[62:63], v[46:47], v[18:19], v[62:63] op_sel_hi:[1,0,1]\n"
    "v_pk_mul_f32 v[64:65], v[48:49], s[80:81] op_sel:[0,1] op_sel_hi:[1,1]\n"
    "buffer_load_dwordx4 v[120:123], v10, s[44:47], s62 offen offset:2048 sc1\n"
    "v_pk_mul_f32 v[66:67], v[50:51], s[80:81] op_sel:[0,1] op_sel_hi:[1,1]\n"
    "v_pk_mul_f32 v[68:69], v[52:53], s[80:81] op_sel:[0,1] op_sel_hi:[1,1]\n"
    "v_pk_mul_f32 v[70:71], v[54:55], s[80:81] op_sel:[0,1] op_sel_hi:[1,1]\n"
    "buffer_load_dwordx4 v[124:127], v10, s[44:47], s62 offen offset:3072 sc1\n"
    "v_pk_mul_f32 v[72:73], v[56:57], s[80:81] op_sel:[0,1] op_sel_hi:[1,1]\n"
    "v_pk_mul_f32 v[74:75], v[58:59], s[80:81] op_sel:[0,1] op_sel_hi:[1,1]\n"
    "v_pk_mul_f32 v[76:77], v[60:61], s[80:81] op_sel:[0,1] op_sel_hi:[1,1]\n"
    "buffer_load_dwordx4 v[128:131], v10, s[44:47], s63 offen offset:0 sc1\n"
    "v_pk_mul_f32 v[78:79], v[62:63], s[80:81] op_sel:[0,1] op_sel_hi:[1,1]\n"
    "v_exp_f32_e64 v64, v64 clamp\n"
    "v_max_f32 v48, s80, v48\n"
    "v_exp_f32_e64 v65, v65 clamp\n"
    "v_max_f32 v49, s80, v49\n"
    "buffer_load_dwordx4 v[132:135], v10, s[44:47], s63 offen offset:1024 sc1\n"
    "v_exp_f32_e64 v66, v66 clamp\n"
    "v_max_f32 v50, s80, v50\n"
    "v_exp_f32_e64 v67, v67 clamp\n"
    "v_max_f32 v51, s80, v51\n"
    "v_exp_f32_e64 v68, v68 clamp\n"
    "v_max_f32 v52, s80, v52\n"
    "buffer_load_dwordx4 v[136:139], v10, s[44:47], s63 offen offset:2048 sc1\n"
    "v_exp_f32_e64 v69, v69 clamp\n"
    "v_max_f32 v53, s80, v53\n"
    "v_exp_f32_e64 v70, v70 clamp\n"
    "v_max_f32 v54, s80, v54\n"
    "v_exp_f32_e64 v71, v71 clamp\n"
    "v_max_f32 v55, s80, v55\n"
    "buffer_load_dwordx4 v[140:143], v10, s[44:47], s63 offen offset:3072 sc1\n"
    "v_exp_f32_e64 v72, v72 clamp\n"
    "v_max_f32 v56, s80, v56\n"
    "v_exp_f32_e64 v73, v73 clamp\n"
    "v_max_f32 v57, s80, v57\n"
    "v_exp_f32_e64 v74, v74 clamp\n"
    "v_max_f32 v58, s80, v58\n"
    "v_exp_f32_e64 v75, v75 clamp\n"
    "v_max_f32 v59, s80, v59\n"
    "v_exp_f32_e64 v76, v76 clamp\n"
    "v_max_f32 v60, s80, v60\n"
    "v_exp_f32_e64 v77, v77 clamp\n"
    "v_max_f32 v61, s80, v61\n"
    "v_exp_f32_e64 v78, v78 clamp\n"
    "v_max_f32 v62, s80, v62\n"
    "v_exp_f32_e64 v79, v79 clamp\n"
    "v_max_f32 v63, s80, v63\n"
    "v_pk_add_f32 v[48:49], v[48:49], v[64:65]\n"
    "v_pk_add_f32 v[50:51], v[50:51], v[66:67]\n"
    "v_pk_add_f32 v[52:53], v[52:53], v[68:69]\n"
    "v_pk_add_f32 v[54:55], v[54:55], v[70:71]\n"
    "v_pk_add_f32 v[56:57], v[56:57], v[72:73]\n"
    "v_pk_add_f32 v[58:59], v[58:59], v[74:75]\n"
    "v_pk_add_f32 v[60:61], v[60:61], v[76:77]\n"
    "v_pk_add_f32 v[62:63], v[62:63], v[78:79]\n"
    "v_cvt_pk_bf16_f32 v64, v48, v49\n"
    "v_cvt_pk_bf16_f32 v65, v50, v51\n"
    "v_cvt_pk_bf16_f32 v66, v52, v53\n"
    "v_cvt_pk_bf16_f32 v67, v54, v55\n"
    "v_cvt_pk_bf16_f32 v68, v56, v57\n"
    "v_cvt_pk_bf16_f32 v69, v58, v59\n"
    "v_cvt_pk_bf16_f32 v70, v60, v61\n"
    "v_cvt_pk_bf16_f32 v71, v62, v63\n"
    "v_cndmask_b32 v70, 0, v70, s[78:79]\n"
    "v_cndmask_b32 v71, 0, v71, s[78:79]\n"
    "s_and_b32 s68, s48, 1\n"
    "s_lshr_b32 s69, s48, 1\n"
    "s_mov_b32 s82, 0\n"
    "L_sweep_6_%=:\n"
    "s_waitcnt vmcnt(15)\n"
    "v_xor_b32 v80, s58, v80\n"
    "s_waitcnt vmcnt(14)\n"
    "v_xor_b32 v84, s58, v84\n"
    "v_or_b32 v11, v80, v84\n"
    "v_pk_add_f32 v[32:33], v[80:81], v[84:85]\n"
    "v_pk_add_f32 v[34:35], v[82:83], v[86:87]\n"
    "s_waitcnt vmcnt(13)\n"
    "v_xor_b32 v88, s58, v88\n"
    "v_pk_add_f32 v[32:33], v[32:33], v[88:89]\n"
    "v_pk_add_f32 v[34:35], v[34:35], v[90:91]\n"
    "s_waitcnt vmcnt(12)\n"
    "v_xor_b32 v92, s58, v92\n"
    "v_or3_b32 v11, v11, v88, v92\n"
    "v_pk_add_f32 v[32:33], v[32:33], v[92:93]\n"
    "v_pk_add_f32 v[34:35], v[34:35], v[94:95]\n"
    "s_waitcnt vmcnt(11)\n"
    "v_xor_b32 v96, s58, v96\n"
    "v_pk_add_f32 v[32:33], v[32:33], v[96:97]\n"
    "v_pk_add_f32 v[34:35], v[34:35], v[98:99]\n"
    "s_waitcnt vmcnt(10)\n"
    "v_xor_b32 v100, s58, v100\n"
    "v_or3_b32 v11, v11, v96, v100\n"
    "v_pk_add_f32 v[32:33], v[32:33], v[100:101]\n"
    "v_pk_add_f32 v[34:35], v[34:35], v[102:103]\n"
    "s_waitcnt vmcnt(9)\n"
    "v_xor_b32 v104, s58, v104\n"
    "v_pk_add_f32 v[32:33], v[32:33], v[104:105]\n"
    "v_pk_add_f32 v[34:35], v[34:35], v[106:107]\n"
    "s_waitcnt vmcnt(8)\n"
    "v_xor_b32 v108, s58, v108\n"
    "v_or3_b32 v11, v11, v104, v108\n"
    "v_pk_add_f32 v[32:33], v[32:33], v[108:109]\n"
    "v_pk_add_f32 v[34:35], v[34:35], v[110:111]\n"
    "s_waitcnt vmcnt(7)\n"
    "v_xor_b32 v112, s58, v112\n"
    "v_pk_add_f32 v[32:33], v[32:33], v[112:113]\n"
    "v_pk_add_f32 v[34:35], v[34:35], v[114:115]\n"
    "s_waitcnt vmcnt(6)\n"
    "v_xor_b32 v116, s58, v116\n"
    "v_or3_b32 v11, v11, v112, v116\n"
    "v_pk_add_f32 v[32:33], v[32:33], v[116:117]\n"
    "v_pk_add_f32 v[34:35], v[34:35], v[118:119]\n"
    "s_waitcnt vmcnt(5)\n"
    "v_xor_b32 v120, s58, v120\n"
    "v_pk_add_f32 v[32:33], v[32:33], v[120:121]\n"
    "v_pk_add_f32 v[34:35], v[34:35], v[122:123]\n"
    "s_waitcnt vmcnt(4)\n"
    "v_xor_b32 v124, s58, v124\n"
    "v_or3_b32 v11, v11, v120, v124\n"
    "v_pk_add_f32 v[32:33], v[32:33], v[124:125]\n"
    "v_pk_add_f32 v[34:35], v[34:35], v[126:127]\n"
    "s_waitcnt vmcnt(3)\n"
    "v_xor_b32 v128, s58, v128\n"
    "v_pk_add_f32 v[32:33], v[32:33], v[128:129]\n"
    "v_pk_add_f32 v[34:35], v[34:35], v[130:131]\n"
    "s_waitcnt vmcnt(2)\n"
    "v_xor_b32 v132, s58, v132\n"
    "v_or3_b32 v11, v11, v128, v132\n"
    "v_pk_add_f32 v[32:33], v[32:33], v[132:133]\n"
    "v_pk_add_f32 v[34:35], v[34:35], v[134:135]\n"
    "s_waitcnt vmcnt(1)\n"
    "v_xor_b32 v136, s58, v136\n"
    "v_pk_add_f32 v[32:33], v[32:33], v[136:137]\n"
    "v_pk_add_f32 v[34:35], v[34:35], v[138:139]\n"
    "s_waitcnt vmcnt(0)\n"
    "v_xor_b32 v140, s58, v140\n"
    "v_or3_b32 v11, v11, v136, v140\n"
    "v_pk_add_f32 v[32:33], v[32:33], v[140:141]\n"
    "v_pk_add_f32 v[34:35], v[34:35], v[142:143]\n"
    "v_and_b32 v11, 0xfff, v11\n"
    "v_cmp_ne_u32 vcc, 0, v11\n"
    "s_nop 0\n"
    "s_cmp_eq_u64 vcc, 0\n"
    "s_cbranch_scc0 L_sretry_7_%=\n"
    "L_sgo_8_%=:\n"
    "s_cmp_eq_u32 s48, 3\n"
    "s_cselect_b32 s67, 0, -1\n"
    "s_mov_b32 s66, -1\n"
    "v_cvt_pk_bf16_f32 v32, v32, v33\n"
    "v_cvt_pk_bf16_f32 v33, v34, v35\n"
    "v_cndmask_b32 v32, 0, v32, s[66:67]\n"
    "v_cndmask_b32 v33, 0, v33, s[66:67]\n"
    "s_lshl_b32 s66, s69, 10\n"
    "s_lshl_b32 s67, s68, 3\n"
    "s_add_u32 s66, s66, s67\n"
    "s_add_u32 s66, s66, 157696\n"
    "v_add_u32 v28, s66, v1\n"
    "v_add_u32 v24, 157696, v1\n"
    "ds_write_b64 v28, v[32:33]\n"
    "s_waitcnt lgkmcnt(0)\n"
    "s_barrier\n"
    "ds_read_b128 v[36:39], v24\n"
    "ds_read_b128 v[40:43], v24 offset:1024\n"
    "s_lshl_b32 s66, s50, 2\n"
    "s_add_u32 s66, s66, s48\n"
    "s_lshl_b32 s66, s66, 4\n"
    "s_add_u32 s66, s66, s49\n"
    "s_lshl_b32 s66, s66, 11\n"
    "s_add_u32 s66, s66, 2097152\n"
    "s_waitcnt lgkmcnt(1)\n"
    "v_mfma_f32_32x32x16_bf16 v[80:95], v[36:39], v[64:67], 0\n"
    "s_waitcnt lgkmcnt(0)\n"
    "v_mfma_f32_32x32x16_bf16 v[80:95], v[40:43], v[68:71], v[80:95]\n"
    "s_lshl_b32 s58, s56, 16\n"
    "s_mov_b32 s57, s56\n"
    "v_mov_b32 v16, s56\n"
    "s_nop 8\n"
    "v_add_f32 v94, s80, v94\n"
    "v_rcp_f32 v94, v94\n"
    "s_nop 0\n"
    "v_pk_mul_f32 v[80:81], v[80:81], v[94:95] op_sel_hi:[1,0]\n"
    "v_pk_mul_f32 v[82:83], v[82:83], v[94:95] op_sel_hi:[1,0]\n"
    "v_pk_mul_f32 v[84:85], v[84:85], v[94:95] op_sel_hi:[1,0]\n"
    "v_pk_mul_f32 v[86:87], v[86:87], v[94:95] op_sel_hi:[1,0]\n"
    "v_pk_mul_f32 v[88:89], v[88:89], v[94:95] op_sel_hi:[1,0]\n"
    "v_pk_mul_f32 v[90:91], v[90:91], v[94:95] op_sel_hi:[1,0]\n"
    "v_pk_mul_f32 v[92:93], v[92:93], v[94:95] op_sel_hi:[1,0]\n"
    "v_cvt_pk_bf16_f32 v96, v80, v81\n"
    "v_cvt_pk_bf16_f32 v97, v82, v83\n"
    "v_cvt_pk_bf16_f32 v98, v84, v85\n"
    "v_cvt_pk_bf16_f32 v99, v86, 0\n"
    "v_cvt_pk_bf16_f32 v100, v87, v88\n"
    "v_cvt_pk_bf16_f32 v101, v89, v90\n"
    "v_cvt_pk_bf16_f32 v102, v91, v92\n"
    "v_cvt_pk_bf16_f32 v103, v93, 0\n"
    "v_or_b32 v99, s58, v99\n"
    "v_or_b32 v103, s58, v103\n"
    "buffer_store_dwordx4 v[96:99], v1, s[44:47], s66 offen sc1\n"
    "buffer_store_dwordx4 v[100:103], v1, s[44:47], s66 offen offset:1024 sc1\n"
    "s_cmp_ge_u32 s55, 28\n"
    "s_cbranch_scc1 L_idle_%=\n"
    "s_lshl_b32 s70, s51, 1\n"
    "s_add_u32 s74, s70, s68\n"
    "v_and_b32 v20, 15, v22\n"
    "v_lshlrev_b32 v20, 10, v20\n"
    "s_lshl_b32 s60, s74, 1\n"
    "s_add_u32 s60, s60, s69\n"
    "s_lshl_b32 s60, s60, 14\n"
    "s_add_u32 s60, s60, 2097152\n"
    "s_mov_b32 s73, s60\n"
    "s_add_u32 s70, s19, 3\n"
    "s_and_b32 s70, s70, 3\n"
    "s_lshl_b32 s70, s70, 12\n"
    "s_add_u32 s63, s60, s70\n"
    "s_add_u32 s70, s19, 2\n"
    "s_and_b32 s70, s70, 3\n"
    "s_lshl_b32 s70, s70, 12\n"
    "s_add_u32 s62, s60, s70\n"
    "s_add_u32 s70, s19, 1\n"
    "s_and_b32 s70, s70, 3\n"
    "s_lshl_b32 s70, s70, 12\n"
    "s_add_u32 s61, s60, s70\n"
    "s_add_u32 s70, s19, 0\n"
    "s_and_b32 s70, s70, 3\n"
    "s_lshl_b32 s70, s70, 12\n"
    "s_add_u32 s60, s60, s70\n"
    "s_add_u32 m0, s83, 94208\n"
    "s_nop 0\n"
    "buffer_load_dwordx4 v3, s[32:35], s64 offen lds\n"
    "v_bfe_u32 v28, v22, 2, 2\n"
    "v_and_b32 v29, 1, v25\n"
    "v_and_b32 v30, 3, v22\n"
    "v_lshlrev_b32 v30, 3, v30\n"
    "v_lshl_add_u32 v31, v29, 5, v30\n"
    "s_mul_i32 s70, s69, 28672\n"
    "s_add_u32 s70, s70, 98304\n"
    "v_mad_u32_u24 v8, v27, 14, v28\n"
    "v_lshlrev_b32 v8, 7, v8\n"
    "s_mul_i32 s64, s86, 56\n"
    "s_add_u32 m0, s83, 40960\n"
    "s_nop 0\n"
    "buffer_load_dwordx4 v0, s[32:35], s64 offen lds\n"
    "v_lshrrev_b32 v24, 1, v28\n"
    "v_xor_b32 v24, v24, v27\n"
    "v_xor_b32 v24, s69, v24\n"
    "v_and_b32 v24, 1, v24\n"
    "buffer_store_dwordx4 a[88:91], v1, s[44:47], s72 offen offset:3072 sc1\n"
    "v_lshl_add_u32 v8, v24, 6, v8\n"
    "v_add3_u32 v8, v8, v31, s70\n"
    "v_xor_b32 v9, 64, v8\n"
    "v_mad_u32_u24 v12, v27, 14, v28\n"
    "s_add_u32 m0, s83, 45056\n"
    "s_nop 0\n"
    "buffer_load_dwordx4 v3, s[32:35], s64 offen lds\n"
    "v_lshlrev_b32 v12, 7, v12\n"
    "v_add_u32 v24, 7, v28\n"
    "v_lshrrev_b32 v24, 1, v24\n"
    "v_xor_b32 v24, v24, v27\n"
    "v_xor_b32 v24, s69, v24\n"
    "v_and_b32 v24, 1, v24\n"
    "v_lshl_add_u32 v12, v24, 6, v12\n"
    "v_add3_u32 v12, v12, v31, s70\n"
    "s_mul_i32 s64, s87, 56\n"
    "s_add_u32 m0, s83, 49152\n"
    "s_nop 0\n"
    "buffer_load_dwordx4 v6, s[32:35], s64 offen lds\n"
    "v_xor_b32 v13, 64, v12\n"
    "v_min_u32 v30, 2, v28\n"
    "v_mad_u32_u24 v14, v27, 14, v30\n"
    "v_lshlrev_b32 v14, 7, v14\n"
    "v_add_u32 v24, 7, v30\n"
    "v_lshrrev_b32 v24, 1, v24\n"
    "v_xor_b32 v24, v24, v27\n"
    "v_xor_b32 v24, s69, v24\n"
    "s_add_u32 m0, s83, 53248\n"
    "s_nop 0\n"
    "buffer_load_dwordx4 v5, s[32:35], s64 offen lds\n"
    "v_and_b32 v24, 1, v24\n"
    "v_lshl_add_u32 v14, v24, 6, v14\n"
    "v_add3_u32 v14, v14, v31, s70\n"
    "v_xor_b32 v15, 64, v14\n"
    "buffer_store_dwordx4 a[88:91], v1, s[44:47], s72 offen offset:2048 sc1\n"
    "s_barrier\n"
    "ds_read_b64_tr_b16 a[96:97], v8 offset:0\n"
    "s_add_u32 m0, s83, 57344\n"
    "s_nop 0\n"
    "buffer_load_dwordx4 v0, s[32:35], s64 offen lds\n"
    "ds_read_b64_tr_b16 a[98:99], v8 offset:512\n"
    "ds_read_b64_tr_b16 a[100:101], v9 offset:0\n"
    "ds_read_b64_tr_b16 a[102:103], v9 offset:512\n"
    "ds_read_b64_tr_b16 a[104:105], v12 offset:896\n"
    "ds_read_b64_tr_b16 a[106:107], v14 offset:1408\n"
    "ds_read_b64_tr_b16 a[108:109], v13 offset:896\n"
    "ds_read_b64_tr_b16 a[110:111], v15 offset:1408\n"
    "ds_read_b64_tr_b16 a[112:113], v8 offset:3584\n"
    "s_add_u32 m0, s83, 61440\n"
    "s_nop 0\n"
    "buffer_load_dwordx4 v3, s[32:35], s64 offen lds\n"
    "ds_read_b64_tr_b16 a[114:115], v8 offset:4096\n"
    "ds_read_b64_tr_b16 a[116:117], v9 offset:3584\n"
    "ds_read_b64_tr_b16 a[118:119], v9 offset:4096\n"
    "ds_read_b64_tr_b16 a[120:121], v12 offset:4480\n"
    "ds_read_b64_tr_b16 a[122:123], v14 offset:4992\n"
    "ds_read_b64_tr_b16 a[124:125], v13 offset:4480\n"
    "ds_read_b64_tr_b16 a[126:127], v15 offset:4992\n"
    "ds_read_b64_tr_b16 a[128:129], v8 offset:7168\n"
    "s_mul_i32 s64, s88, 56\n"
    "s_add_u32 m0, s83, 65536\n"
    "s_nop 0\n"
    "buffer_load_dwordx4 v6, s[32:35], s64 offen lds\n"
    "ds_read_b64_tr_b16 a[130:131], v8 offset:7680\n"
    "ds_read_b64_tr_b16 a[132:133], v9 offset:7168\n"
    "ds_read_b64_tr_b16 a[134:135], v9 offset:7680\n"
    "ds_read_b64_tr_b16 a[136:137], v12 offset:8064\n"
    "ds_read_b64_tr_b16 a[138:139], v14 offset:8576\n"
    "ds_read_b64_tr_b16 a[140:141], v13 offset:8064\n"
    "ds_read_b64_tr_b16 a[142:143], v15 offset:8576\n"
    "ds_read_b64_tr_b16 a[144:145], v8 offset:10752\n"
    "s_add_u32 m0, s83, 69632\n"
    "s_nop 0\n"
    "buffer_load_dwordx4 v5, s[32:35], s64 offen lds\n"
    "ds_read_b64_tr_b16 a[146:147], v8 offset:11264\n"
    "ds_read_b64_tr_b16 a[148:149], v9 offset:10752\n"
    "ds_read_b64_tr_b16 a[150:151], v9 offset:11264\n"
    "ds_read_b64_tr_b16 a[152:153], v12 offset:11648\n"
    "ds_read_b64_tr_b16 a[154:155], v14 offset:12160\n"
    "ds_read_b64_tr_b16 a[156:157], v13 offset:11648\n"
    "ds_read_b64_tr_b16 a[158:159], v15 offset:12160\n"
    "ds_read_b64_tr_b16 a[160:161], v8 offset:14336\n"
    "ds_read_b64_tr_b16 a[162:163], v8 offset:14848\n"
    "ds_read_b64_tr_b16 a[164:165], v9 offset:14336\n"
    "ds_read_b64_tr_b16 a[166:167], v9 offset:14848\n"
    "ds_read_b64_tr_b16 a[168:169], v12 offset:15232\n"
    "ds_read_b64_tr_b16 a[170:171], v14 offset:15744\n"
    "ds_read_b64_tr_b16 a[172:173], v13 offset:15232\n"
    "ds_read_b64_tr_b16 a[174:175], v15 offset:15744\n"
    "ds_read_b64_tr_b16 a[176:177], v8 offset:17920\n"
    "ds_read_b64_tr_b16 a[178:179], v8 offset:18432\n"
    "ds_read_b64_tr_b16 a[180:181], v9 offset:17920\n"
    "ds_read_b64_tr_b16 a[182:183], v9 offset:18432\n"
    "ds_read_b64_tr_b16 a[184:185], v12 offset:18816\n"
    "ds_read_b64_tr_b16 a[186:187], v14 offset:19328\n"
    "ds_read_b64_tr_b16 a[188:189], v13 offset:18816\n"
    "ds_read_b64_tr_b16 a[190:191], v15 offset:19328\n"
    "ds_read_b64_tr_b16 a[192:193], v8 offset:21504\n"
    "ds_read_b64_tr_b16 a[194:195], v8 offset:22016\n"
    "ds_read_b64_tr_b16 a[196:197], v9 offset:21504\n"
    "ds_read_b64_tr_b16 a[198:199], v9 offset:22016\n"
    "ds_read_b64_tr_b16 a[200:201], v12 offset:22400\n"
    "ds_read_b64_tr_b16 a[202:203], v14 offset:22912\n"
    "ds_read_b64_tr_b16 a[204:205], v13 offset:22400\n"
    "ds_read_b64_tr_b16 a[206:207], v15 offset:22912\n"
    "ds_read_b64_tr_b16 a[208:209], v8 offset:25088\n"
    "ds_read_b64_tr_b16 a[210:211], v8 offset:25600\n"
    "ds_read_b64_tr_b16 a[212:213], v9 offset:25088\n"
    "ds_read_b64_tr_b16 a[214:215], v9 offset:25600\n"
    "ds_read_b64_tr_b16 a[216:217], v12 offset:25984\n"
    "ds_read_b64_tr_b16 a[218:219], v14 offset:26496\n"
    "ds_read_b64_tr_b16 a[220:221], v13 offset:25984\n"
    "ds_read_b64_tr_b16 a[222:223], v15 offset:26496\n"
    "v_bfe_u32 v28, v22, 2, 2\n"
    "v_and_b32 v29, 1, v25\n"
    "v_lshl_add_u32 v30, v27, 2, v28\n"
    "v_lshlrev_b32 v30, 7, v30\n"
    "v_lshl_add_u32 v8, v29, 11, v30\n"
    "v_lshrrev_b32 v31, 1, v28\n"
    "v_lshl_add_u32 v24, v29, 1, v31\n"
    "v_lshl_add_u32 v8, v24, 5, v8\n"
    "v_and_b32 v24, 3, v22\n"
    "v_lshl_add_u32 v8, v24, 3, v8\n"
    "v_xor_b32 v9, 32, v8\n"
    "v_add_u32 v10, 57344, v8\n"
    "v_add_u32 v11, 57344, v9\n"
    "s_lshl_b32 s70, s48, 5\n"
    "v_xor_b32 v12, s70, v8\n"
    "v_xor_b32 v13, s70, v10\n"
    "s_mul_i32 s75, s48, 10240\n"
    "s_add_u32 s75, s75, 114688\n"
    "v_xor_b32 v31, s69, v31\n"
    "v_lshl_add_u32 v30, v31, 6, v30\n"
    "v_xor_b32 v31, s68, v29\n"
    "v_lshl_add_u32 v30, v31, 5, v30\n"
    "v_lshl_add_u32 v30, v24, 3, v30\n"
    "v_add_u32 v136, s75, v30\n"
    "v_xor_b32 v137, 64, v136\n"
    "s_mul_i32 s70, s48, 401408\n"
    "s_lshl_b32 s71, s52, 7\n"
    "s_add_u32 s70, s70, s71\n"
    "v_add_u32 v4, s70, v4\n"
    "v_add_u32 v143, 6144, v4\n"
    "s_cmp_eq_u32 %1, 0\n"
    "s_cselect_b32 s71, 0, 2147483392\n"
    "v_max_u32 v15, s71, v2\n"
    "v_add_u32 v18, s75, v1\n"
    "s_xor_b32 s70, s48, 2\n"
    "s_mul_i32 s70, s70, 10240\n"
    "s_add_u32 s70, s70, 114688\n"
    "v_add_u32 v19, s70, v1\n"
    "s_mul_i32 s4, s74, 28\n"
    "s_lshl_b32 s70, s52, 1\n"
    "s_add_u32 s70, s70, s69\n"
    "s_lshl_b32 s65, s70, 1\n"
    "s_add_u32 s4, s4, s65\n"
    "s_lshl_b32 s4, s4, 10\n"
    "s_add_u32 s4, s4, 4194304\n"
    "s_lshr_b32 s65, s70, 2\n"
    "s_lshl_b32 s65, s65, 10\n"
    "s_and_b32 s70, s70, 3\n"
    "s_lshl_b32 s70, s70, 2\n"
    "s_add_u32 s65, s65, s70\n"
    "s_lshl_b32 s70, s74, 12\n"
    "s_add_u32 s65, s65, s70\n"
    "s_add_u32 s65, s65, 262144\n"
    "s_mov_b32 s66, 4096\n"
    "v_accvgpr_read_b32 v80, a72\n"
    "v_accvgpr_read_b32 v81, a73\n"
    "v_accvgpr_read_b32 v82, a74\n"
    "v_accvgpr_read_b32 v83, a75\n"
    "v_accvgpr_read_b32 v84, a76\n"
    "v_accvgpr_read_b32 v85, a77\n"
    "v_accvgpr_read_b32 v86, a78\n"
    "v_accvgpr_read_b32 v87, a79\n"
    "v_permlane32_swap_b32 v80, v82\n"
    "v_permlane32_swap_b32 v81, v83\n"
    "v_permlane32_swap_b32 v84, v86\n"
    "v_permlane32_swap_b32 v85, v87\n"
    "v_lshlrev_b32 v32, 16, v80\n"
    "v_and_b32 v33, 0xffff0000, v80\n"
    "v_lshlrev_b32 v34, 16, v81\n"
    "v_and_b32 v35, 0xffff0000, v81\n"
    "v_lshlrev_b32 v36, 16, v82\n"
    "v_and_b32 v37, 0xffff0000, v82\n"
    "v_lshlrev_b32 v38, 16, v83\n"
    "v_and_b32 v39, 0xffff0000, v83\n"
    "v_lshlrev_b32 v40, 16, v84\n"
    "v_and_b32 v41, 0xffff0000, v84\n"
    "v_lshlrev_b32 v42, 16, v85\n"
    "v_and_b32 v43, 0xffff0000, v85\n"
    "v_lshlrev_b32 v44, 16, v86\n"
    "v_and_b32 v45, 0xffff0000, v86\n"
    "v_lshlrev_b32 v46, 16, v87\n"
    "v_and_b32 v47, 0xffff0000, v87\n"
    "buffer_load_dword v21, v20, s[44:47], s73 offen offset:908 sc1\n"
    "buffer_load_dwordx4 v[144:147], v1, s[44:47], s60 offen offset:0 sc1\n"
    "v_mov_b32 v112, s59\n"
    "buffer_load_dwordx4 v[148:151], v1, s[44:47], s60 offen offset:1024 sc1\n"
    "v_mov_b32 v113, s59\n"
    "buffer_load_dwordx4 v[152:155], v1, s[44:47], s60 offen offset:2048 sc1\n"
    "v_mov_b32 v114, s59\n"
    "buffer_load_dwordx4 v[156:159], v1, s[44:47], s60 offen offset:3072 sc1\n"
    "v_mov_b32 v115, s59\n"
    "buffer_load_dwordx4 v[160:163], v1, s[44:47], s61 offen offset:0 sc1\n"
    "s_waitcnt lgkmcnt(0)\n"
    "buffer_load_dwordx4 v[164:167], v1, s[44:47], s61 offen offset:1024 sc1\n"
    "s_barrier\n"
    "buffer_load_dwordx4 v[168:171], v1, s[44:47], s61 offen offset:2048 sc1\n"
    "buffer_load_dwordx4 v[172:175], v1, s[44:47], s61 offen offset:3072 sc1\n"
    "buffer_load_dwordx4 v[176:179], v1, s[44:47], s62 offen offset:0 sc1\n"
    "buffer_load_dwordx4 v[180:183], v1, s[44:47], s62 offen offset:1024 sc1\n"
    "s_waitcnt vmcnt(10)\n"
    "v_xor_b32 v21, s58, v21\n"
    "v_cmp_lt_u32 vcc, 0xffff, v21\n"
    "s_cbranch_vccnz L_retry_9_%=\n"
    "L_go_10_%=:\n"
    "s_waitcnt vmcnt(9)\n"
    "v_xor_b32 v147, s58, v147\n"
    "s_nop 1\n"
    "v_mfma_f32_32x32x16_bf16 v[48:63], a[100:103], v[144:147], 0\n"
    "buffer_load_dwordx4 v[184:187], v1, s[44:47], s62 offen offset:2048 sc1\n"
    "s_waitcnt vmcnt(9)\n"
    "v_xor_b32 v151, s58, v151\n"
    "v_mfma_f32_32x32x16_bf16 v[32:47], a[96:99], v[144:147], v[32:47]\n"
    "v_or_b32 v29, v147, v151\n"
    "v_mfma_f32_32x32x16_bf16 v[48:63], a[108:111], v[148:151], v[48:63]\n"
    "buffer_load_dwordx4 v[188:191], v1, s[44:47], s62 offen offset:3072 sc1\n"
    "s_waitcnt vmcnt(9)\n"
    "v_xor_b32 v155, s58, v155\n"
    "v_mfma_f32_32x32x16_bf16 v[32:47], a[104:107], v[148:151], v[32:47]\n"
    "s_nop 0\n"
    "v_mfma_f32_32x32x16_bf16 v[48:63], a[116:119], v[152:155], v[48:63]\n"
    "buffer_load_dwordx4 v[192:195], v1, s[44:47], s63 offen offset:0 sc1\n"
    "s_waitcnt vmcnt(9)\n"
    "v_xor_b32 v159, s58, v159\n"
    "v_mfma_f32_32x32x16_bf16 v[32:47], a[112:115], v[152:155], v[32:47]\n"
    "v_or3_b32 v29, v29, v155, v159\n"
    "v_mfma_f32_32x32x16_bf16 v[48:63], a[124:127], v[156:159], v[48:63]\n"
    "buffer_load_dwordx4 v[196:199], v1, s[44:47], s63 offen offset:1024 sc1\n"
    "s_waitcnt vmcnt(9)\n"
    "v_xor_b32 v163, s58, v163\n"
    "v_mfma_f32_32x32x16_bf16 v[32:47], a[120:123], v[156:159], v[32:47]\n"
    "s_nop 0\n"
    "v_mfma_f32_32x32x16_bf16 v[48:63], a[132:135], v[160:163], v[48:63]\n"
    "buffer_load_dwordx4 v[200:203], v1, s[44:47], s63 offen offset:2048 sc1\n"
    "s_waitcnt vmcnt(9)\n"
    "v_xor_b32 v167, s58, v167\n"
    "v_mfma_f32_32x32x16_bf16 v[32:47], a[128:131], v[160:163], v[32:47]\n"
    "v_or3_b32 v29, v29, v163, v167\n"
    "v_mfma_f32_32x32x16_bf16 v[48:63], a[140:143], v[164:167], v[48:63]\n"
    "buffer_load_dwordx4 v[204:207], v1, s[44:47], s63 offen offset:3072 sc1\n"
    "s_waitcnt vmcnt(9)\n"
    "v_xor_b32 v171, s58, v171\n"
    "v_mfma_f32_32x32x16_bf16 v[32:47], a[136:139], v[164:167], v[32:47]\n"
    "s_nop 0\n"
    "v_mfma_f32_32x32x16_bf16 v[48:63], a[148:151], v[168:171], v[48:63]\n"
    "s_waitcnt vmcnt(8)\n"
    "v_xor_b32 v175, s58, v175\n"
    "s_add_u32 m0, s83, 73728\n"
    "s_nop 0\n"
    "buffer_load_dwordx4 v0, s[32:35], s64 offen lds\n"
    "s_add_u32 m0, s83, 77824\n"
    "s_nop 0\n"
    "buffer_load_dwordx4 v3, s[32:35], s64 offen lds\n"
    "v_mfma_f32_32x32x16_bf16 v[32:47], a[144:147], v[168:171], v[32:47]\n"
    "v_or3_b32 v29, v29, v171, v175\n"
    "s_mul_i32 s64, s89, 56\n"
    "s_add_u32 m0, s83, 81920\n"
    "s_nop 0\n"
    "buffer_load_dwordx4 v6, s[32:35], s64 offen lds\n"
    "v_mfma_f32_32x32x16_bf16 v[48:63], a[156:159], v[172:175], v[48:63]\n"
    "s_waitcnt vmcnt(10)\n"
    "v_xor_b32 v179, s58, v179\n"
    "s_mul_i32 s64, s90, 56\n"
    "s_add_u32 m0, s83, 98304\n"
    "s_nop 0\n"
    "buffer_load_dwordx4 v6, s[32:35], s64 offen lds\n"
    "v_mfma_f32_32x32x16_bf16 v[32:47], a[152:155], v[172:175], v[32:47]\n"
    "s_add_u32 m0, s83, 102400\n"
    "s_nop 0\n"
    "buffer_load_dwordx4 v5, s[32:35], s64 offen lds\n"
    "v_mfma_f32_32x32x16_bf16 v[48:63], a[164:167], v[176:179], v[48:63]\n"
    "s_waitcnt vmcnt(11)\n"
    "v_xor_b32 v183, s58, v183\n"
    "s_add_u32 m0, s83, 106496\n"
    "s_nop 0\n"
    "buffer_load_dwordx4 v0, s[32:35], s64 offen lds\n"
    "v_mfma_f32_32x32x16_bf16 v[32:47], a[160:163], v[176:179], v[32:47]\n"
    "v_or3_b32 v29, v29, v179, v183\n"
    "s_add_u32 m0, s83, 110592\n"
    "s_nop 0\n"
    "buffer_load_dwordx4 v3, s[32:35], s64 offen lds\n"
    "v_mfma_f32_32x32x16_bf16 v[48:63], a[172:175], v[180:183], v[48:63]\n"
    "s_waitcnt vmcnt(12)\n"
    "v_xor_b32 v187, s58, v187\n"
    "s_mul_i32 s5, s84, 14\n"
    "s_add_u32 m0, s75, 4096\n"
    "s_nop 0\n"
    "buffer_load_dwordx4 v4, s[36:39], s5 offen lds\n"
    "v_mfma_f32_32x32x16_bf16 v[32:47], a[168:171], v[180:183], v[32:47]\n"
    "buffer_load_dwordx4 v143, s[36:39], s5 offen offset:1024 lds\n"
    "v_mfma_f32_32x32x16_bf16 v[48:63], a[180:183], v[184:187], v[48:63]\n"
    "s_waitcnt vmcnt(13)\n"
    "v_xor_b32 v191, s58, v191\n"
    "s_add_u32 s5, s5, 14336\n"
    "s_add_u32 m0, s75, 6144\n"
    "s_nop 0\n"
    "buffer_load_dwordx4 v4, s[36:39], s5 offen lds\n"
    "v_mfma_f32_32x32x16_bf16 v[32:47], a[176:179], v[184:187], v[32:47]\n"
    "v_or3_b32 v29, v29, v187, v191\n"
    "buffer_load_dwordx4 v143, s[36:39], s5 offen offset:1024 lds\n"
    "v_mfma_f32_32x32x16_bf16 v[48:63], a[188:191], v[188:191], v[48:63]\n"
    "s_waitcnt vmcnt(14)\n"
    "v_xor_b32 v195, s58, v195\n"
    "s_mul_i32 s5, s85, 14\n"
    "s_add_u32 m0, s75, 8192\n"
    "s_nop 0\n"
    "buffer_load_dwordx4 v4, s[36:39], s5 offen lds\n"
    "v_mfma_f32_32x32x16_bf16 v[32:47], a[184:187], v[188:191], v[32:47]\n"
    "buffer_load_dwordx4 v143, s[36:39], s5 offen offset:1024 lds\n"
    "v_mfma_f32_32x32x16_bf16 v[48:63], a[196:199], v[192:195], v[48:63]\n"
    "s_waitcnt vmcnt(15)\n"
    "v_xor_b32 v199, s58, v199\n"
    "buffer_store_dwordx4 a[88:91], v1, s[44:47], s72 offen offset:1024 sc1\n"
    "v_mfma_f32_32x32x16_bf16 v[32:47], a[192:195], v[192:195], v[32:47]\n"
    "v_or3_b32 v29, v29, v195, v199\n"
    "s_waitcnt vmcnt(15)\n"
    "v_xor_b32 v203, s58, v203\n"
    "v_mfma_f32_32x32x16_bf16 v[48:63], a[204:207], v[196:199], v[48:63]\n"
    "s_waitcnt vmcnt(14)\n"
    "v_xor_b32 v207, s58, v207\n"
    "v_mfma_f32_32x32x16_bf16 v[48:63], a[212:215], v[200:203], v[48:63]\n"
    "v_or3_b32 v29, v29, v203, v207\n"
    "v_mfma_f32_32x32x16_bf16 v[48:63], a[220:223], v[204:207], v[48:63]\n"
    "buffer_store_dwordx4 a[88:91], v1, s[44:47], s72 offen offset:0 sc1\n"
    "v_mfma_f32_32x32x16_bf16 v[32:47], a[200:203], v[196:199], v[32:47]\n"
    "v_mfma_f32_32x32x16_bf16 v[32:47], a[208:211], v[200:203], v[32:47]\n"
    "s_nop 8\n"
    "ds_write_b128 v18, v[48:51] offset:0\n"
    "ds_write_b128 v18, v[52:55] offset:1024\n"
    "v_mfma_f32_32x32x16_bf16 v[32:47], a[216:219], v[204:207], v[32:47]\n"
    "ds_write_b128 v18, v[56:59] offset:2048\n"
    "ds_write_b128 v18, v[60:63] offset:3072\n"
    "v_cmp_lt_u32 vcc, 0xffff, v29\n"
    "s_cbranch_vccnz L_redo_13_%=\n"
    "L_go_14_%=:\n"
    "s_waitcnt lgkmcnt(0)\n"
    "s_barrier\n"
    "ds_read_b128 v[64:67], v19 offset:0\n"
    "ds_read_b128 v[68:71], v19 offset:1024\n"
    "ds_read_b128 v[72:75], v19 offset:2048\n"
    "ds_read_b128 v[76:79], v19 offset:3072\n"
    "buffer_store_dword v16, v15, s[44:47], 0 offen sc1\n"
    "s_waitcnt lgkmcnt(3)\n"
    "v_pk_add_f32 v[32:33], v[32:33], v[64:65]\n"
    "v_pk_add_f32 v[34:35], v[34:35], v[66:67]\n"
    "s_waitcnt lgkmcnt(2)\n"
    "v_pk_add_f32 v[36:37], v[36:37], v[68:69]\n"
    "v_pk_add_f32 v[38:39], v[38:39], v[70:71]\n"
    "s_waitcnt lgkmcnt(1)\n"
    "v_pk_add_f32 v[40:41], v[40:41], v[72:73]\n"
    "v_pk_add_f32 v[42:43], v[42:43], v[74:75]\n"
    "s_waitcnt lgkmcnt(0)\n"
    "v_pk_add_f32 v[44:45], v[44:45], v[76:77]\n"
    "v_pk_add_f32 v[46:47], v[46:47], v[78:79]\n"
    "v_cvt_pk_bf16_f32 v88, v32, v33\n"
    "v_cvt_pk_bf16_f32 v89, v34, v35\n"
    "v_cvt_pk_bf16_f32 v90, v36, v37\n"
    "v_cvt_pk_bf16_f32 v91, v38, v39\n"
    "v_cvt_pk_bf16_f32 v92, v40, v41\n"
    "v_cvt_pk_bf16_f32 v93, v42, v43\n"
    "v_cvt_pk_bf16_f32 v94, v44, v45\n"
    "v_cvt_pk_bf16_f32 v95, v46, v47\n"
    "buffer_store_dwordx4 v[88:91], v1, s[44:47], s4 offen sc1\n"
    "buffer_store_dwordx4 v[92:95], v1, s[44:47], s4 offen offset:1024 sc1\n"
    "v_pk_mul_f32 v[66:67], v[32:33], v[32:33]\n"
    "v_pk_add_f32 v[64:65], v[32:33], v[34:35]\n"
    "v_pk_fma_f32 v[66:67], v[34:35], v[34:35], v[66:67]\n"
    "v_pk_add_f32 v[64:65], v[64:65], v[36:37]\n"
    "v_pk_fma_f32 v[66:67], v[36:37], v[36:37], v[66:67]\n"
    "v_pk_add_f32 v[64:65], v[64:65], v[38:39]\n"
    "v_pk_fma_f32 v[66:67], v[38:39], v[38:39], v[66:67]\n"
    "v_pk_add_f32 v[64:65], v[64:65], v[40:41]\n"
    "v_pk_fma_f32 v[66:67], v[40:41], v[40:41], v[66:67]\n"
    "v_pk_add_f32 v[64:65], v[64:65], v[42:43]\n"
    "v_pk_fma_f32 v[66:67], v[42:43], v[42:43], v[66:67]\n"
    "v_pk_add_f32 v[64:65], v[64:65], v[44:45]\n"
    "v_pk_fma_f32 v[66:67], v[44:45], v[44:45], v[66:67]\n"
    "v_pk_add_f32 v[64:65], v[64:65], v[46:47]\n"
    "v_pk_fma_f32 v[66:67], v[46:47], v[46:47], v[66:67]\n"
    "v_add_f32 v64, v64, v65\n"
    "v_add_f32 v66, v66, v67\n"
    "s_nop 1\n"
    "v_permlane32_swap_b32 v64, v66\n"
    "v_add_f32 v64, v64, v66\n"
    "buffer_store_dword v64, v1, s[44:47], s65 offen sc1\n"
    "v_min_u32 v14, 6, v22\n"
    "v_lshlrev_b32 v14, 7, v14\n"
    "s_lshl_b32 s4, s51, 10\n"
    "s_add_u32 s4, s4, 65536\n"
    "s_lshl_b32 s66, s52, 7\n"
    "s_add_u32 s66, s66, s4\n"
    "s_lshl_b32 s74, s53, 2\n"
    "s_add_u32 s74, s74, s48\n"
    "s_mul_i32 s73, s74, 57344\n"
    "s_add_u32 s73, s73, 4194304\n"
    "v_add_u32 v139, s73, v1\n"
    "v_add_u32 v140, 28672, v139\n"
    "s_lshl_b32 s73, s74, 10\n"
    "s_add_u32 s73, s73, 65536\n"
    "s_lshl_b32 s61, s74, 13\n"
    "s_add_u32 s61, s61, 262144\n"
    "s_add_u32 s62, s61, 4096\n"
    "s_mul_i32 s63, s74, 229376\n"
    "s_lshl_b32 s71, s54, 12\n"
    "s_add_u32 s63, s63, s71\n"
    "s_add_u32 s63, s63, 6291456\n"
    "s_add_u32 s64, s63, 114688\n"
    "ds_read_b64_tr_b16 a[224:225], v12 offset:0\n"
    "ds_read_b64_tr_b16 a[226:227], v12 offset:1024\n"
    "ds_read_b64_tr_b16 a[228:229], v12 offset:4096\n"
    "ds_read_b64_tr_b16 a[230:231], v12 offset:5120\n"
    "ds_read_b64_tr_b16 a[232:233], v12 offset:8192\n"
    "ds_read_b64_tr_b16 a[234:235], v12 offset:9216\n"
    "s_waitcnt lgkmcnt(4)\n"
    "v_mfma_f32_32x32x16_bf16 v[96:111], v[112:115], a[224:227], 0\n"
    "ds_read_b64_tr_b16 a[236:237], v12 offset:12288\n"
    "ds_read_b64_tr_b16 a[238:239], v12 offset:13312\n"
    "s_waitcnt vmcnt(10)\n"
    "ds_read_b64_tr_b16 a[0:1], v136 offset:4096\n"
    "ds_read_b64_tr_b16 a[2:3], v136 offset:5120\n"
    "s_waitcnt lgkmcnt(6)\n"
    "v_mfma_f32_32x32x16_bf16 v[96:111], v[112:115], a[228:231], v[96:111]\n"
    "ds_read_b64_tr_b16 a[240:241], v12 offset:16384\n"
    "ds_read_b64_tr_b16 a[242:243], v12 offset:17408\n"
    "ds_read_b64_tr_b16 a[4:5], v137 offset:4096\n"
    "ds_read_b64_tr_b16 a[6:7], v137 offset:5120\n"
    "s_waitcnt lgkmcnt(8)\n"
    "v_mfma_f32_32x32x16_bf16 v[96:111], v[112:115], a[232:235], v[96:111]\n"
    "ds_read_b64_tr_b16 a[244:245], v12 offset:20480\n"
    "ds_read_b64_tr_b16 a[246:247], v12 offset:21504\n"
    "s_waitcnt lgkmcnt(8)\n"
    "v_mfma_f32_32x32x16_bf16 v[96:111], v[112:115], a[236:239], v[96:111]\n"
    "ds_read_b64_tr_b16 a[248:249], v12 offset:24576\n"
    "ds_read_b64_tr_b16 a[250:251], v12 offset:25600\n"
    "s_waitcnt lgkmcnt(4)\n"
    "s_add_u32 s5, s5, 14336\n"
    "s_add_u32 m0, s75, 4096\n"
    "s_nop 0\n"
    "buffer_load_dwordx4 v4, s[36:39], s5 offen lds\n"
    "v_mfma_f32_32x32x16_bf16 v[96:111], v[112:115], a[240:243], v[96:111]\n"
    "ds_read_b64_tr_b16 a[252:253], v12 offset:28672\n"
    "ds_read_b64_tr_b16 a[254:255], v12 offset:29696\n"
    "buffer_load_dwordx4 v143, s[36:39], s5 offen offset:1024 lds\n"
    "s_waitcnt vmcnt(10)\n"
    "ds_read_b64_tr_b16 a[8:9], v136 offset:6144\n"
    "ds_read_b64_tr_b16 a[10:11], v136 offset:7168\n"
    "s_waitcnt lgkmcnt(6)\n"
    "v_mfma_f32_32x32x16_bf16 v[96:111], v[112:115], a[244:247], v[96:111]\n"
    "ds_read_b64_tr_b16 a[224:225], v12 offset:32768\n"
    "ds_read_b64_tr_b16 a[226:227], v12 offset:33792\n"
    "ds_read_b64_tr_b16 a[12:13], v137 offset:6144\n"
    "ds_read_b64_tr_b16 a[14:15], v137 offset:7168\n"
    "s_waitcnt lgkmcnt(8)\n"
    "v_mfma_f32_32x32x16_bf16 v[96:111], v[112:115], a[248:251], v[96:111]\n"
    "ds_read_b64_tr_b16 a[228:229], v12 offset:36864\n"
    "ds_read_b64_tr_b16 a[230:231], v12 offset:37888\n"
    "s_waitcnt lgkmcnt(8)\n"
    "v_mfma_f32_32x32x16_bf16 v[96:111], v[112:115], a[252:255], v[96:111]\n"
    "ds_read_b64_tr_b16 a[232:233], v13 offset:28672\n"
    "ds_read_b64_tr_b16 a[234:235], v13 offset:29696\n"
    "s_waitcnt lgkmcnt(4)\n"
    "s_mul_i32 s5, s86, 14\n"
    "s_add_u32 m0, s75, 6144\n"
    "s_nop 0\n"
    "buffer_load_dwordx4 v4, s[36:39], s5 offen lds\n"
    "v_mfma_f32_32x32x16_bf16 v[96:111], v[112:115], a[224:227], v[96:111]\n"
    "ds_read_b64_tr_b16 a[236:237], v13 offset:32768\n"
    "ds_read_b64_tr_b16 a[238:239], v13 offset:33792\n"
    "buffer_load_dwordx4 v143, s[36:39], s5 offen offset:1024 lds\n"
    "s_waitcnt vmcnt(10)\n"
    "ds_read_b64_tr_b16 a[16:17], v136 offset:8192\n"
    "ds_read_b64_tr_b16 a[18:19], v136 offset:9216\n"
    "s_waitcnt lgkmcnt(6)\n"
    "v_mfma_f32_32x32x16_bf16 v[96:111], v[112:115], a[228:231], v[96:111]\n"
    "ds_read_b64_tr_b16 a[240:241], v13 offset:36864\n"
    "ds_read_b64_tr_b16 a[242:243], v13 offset:37888\n"
    "ds_read_b64_tr_b16 a[20:21], v137 offset:8192\n"
    "ds_read_b64_tr_b16 a[22:23], v137 offset:9216\n"
    "s_waitcnt lgkmcnt(8)\n"
    "v_mfma_f32_32x32x16_bf16 v[96:111], v[112:115], a[232:235], v[96:111]\n"
    "ds_read_b64_tr_b16 a[244:245], v12 offset:40960\n"
    "ds_read_b64_tr_b16 a[246:247], v12 offset:41984\n"
    "s_waitcnt lgkmcnt(8)\n"
    "v_mfma_f32_32x32x16_bf16 v[96:111], v[112:115], a[236:239], v[96:111]\n"
    "ds_read_b64_tr_b16 a[248:249], v12 offset:45056\n"
    "ds_read_b64_tr_b16 a[250:251], v12 offset:46080\n"
    "s_waitcnt lgkmcnt(4)\n"
    "s_add_u32 s5, s5, 14336\n"
    "s_add_u32 m0, s75, 8192\n"
    "s_nop 0\n"
    "buffer_load_dwordx4 v4, s[36:39], s5 offen lds\n"
    "v_mfma_f32_32x32x16_bf16 v[96:111], v[112:115], a[240:243], v[96:111]\n"
    "ds_read_b64_tr_b16 a[252:253], v12 offset:49152\n"
    "ds_read_b64_tr_b16 a[254:255], v12 offset:50176\n"
    "s_waitcnt vmcnt(5)\n"
    "s_barrier\n"
    "buffer_store_dword v16, v2, s[44:47], s66 offen sc1\n"
    "buffer_load_dwordx4 v143, s[36:39], s5 offen offset:1024 lds\n"
    "s_waitcnt lgkmcnt(4)\n"
    "v_mfma_f32_32x32x16_bf16 v[96:111], v[112:115], a[244:247], v[96:111]\n"
    "ds_read_b64_tr_b16 a[224:225], v12 offset:53248\n"
    "ds_read_b64_tr_b16 a[226:227], v12 offset:54272\n"
    "s_waitcnt lgkmcnt(4)\n"
    "v_mfma_f32_32x32x16_bf16 v[96:111], v[112:115], a[248:251], v[96:111]\n"
    "ds_read_b64_tr_b16 a[228:229], v13 offset:0\n"
    "ds_read_b64_tr_b16 a[230:231], v13 offset:1024\n"
    "s_waitcnt lgkmcnt(4)\n"
    "v_mfma_f32_32x32x16_bf16 v[96:111], v[112:115], a[252:255], v[96:111]\n"
    "ds_read_b64_tr_b16 a[232:233], v13 offset:4096\n"
    "ds_read_b64_tr_b16 a[234:235], v13 offset:5120\n"
    "s_waitcnt lgkmcnt(4)\n"
    "v_mfma_f32_32x32x16_bf16 v[96:111], v[112:115], a[224:227], v[96:111]\n"
    "ds_read_b64_tr_b16 a[236:237], v13 offset:8192\n"
    "ds_read_b64_tr_b16 a[238:239], v13 offset:9216\n"
    "s_mul_i32 s5, s87, 14\n"
    "s_add_u32 m0, s75, 0\n"
    "s_nop 0\n"
    "buffer_load_dwordx4 v4, s[36:39], s5 offen lds\n"
    "s_waitcnt lgkmcnt(4)\n"
    "v_mfma_f32_32x32x16_bf16 v[96:111], v[112:115], a[228:231], v[96:111]\n"
    "ds_read_b64_tr_b16 a[240:241], v13 offset:12288\n"
    "ds_read_b64_tr_b16 a[242:243], v13 offset:13312\n"
    "buffer_load_dwordx4 v143, s[36:39], s5 offen offset:1024 lds\n"
    "s_waitcnt lgkmcnt(4)\n"
    "v_mfma_f32_32x32x16_bf16 v[96:111], v[112:115], a[232:235], v[96:111]\n"
    "ds_read_b64_tr_b16 a[244:245], v13 offset:16384\n"
    "ds_read_b64_tr_b16 a[246:247], v13 offset:17408\n"
    "s_waitcnt lgkmcnt(4)\n"
    "v_mfma_f32_32x32x16_bf16 v[96:111], v[112:115], a[236:239], v[96:111]\n"
    "ds_read_b64_tr_b16 a[248:249], v13 offset:20480\n"
    "ds_read_b64_tr_b16 a[250:251], v13 offset:21504\n"
    "s_add_u32 s5, s5, 14336\n"
    "s_add_u32 m0, s75, 2048\n"
    "s_nop 0\n"
    "buffer_load_dwordx4 v4, s[36:39], s5 offen lds\n"
    "s_waitcnt lgkmcnt(4)\n"
    "v_mfma_f32_32x32x16_bf16 v[96:111], v[112:115], a[240:243], v[96:111]\n"
    "ds_read_b64_tr_b16 a[252:253], v13 offset:24576\n"
    "ds_read_b64_tr_b16 a[254:255], v13 offset:25600\n"
    "buffer_load_dwordx4 v143, s[36:39], s5 offen offset:1024 lds\n"
    "s_waitcnt vmcnt(9)\n"
    "ds_read_b64_tr_b16 a[24:25], v136 offset:4096\n"
    "ds_read_b64_tr_b16 a[26:27], v136 offset:5120\n"
    "s_waitcnt lgkmcnt(6)\n"
    "v_mfma_f32_32x32x16_bf16 v[96:111], v[112:115], a[244:247], v[96:111]\n"
    "ds_read_b64_tr_b16 a[224:225], v13 offset:40960\n"
    "ds_read_b64_tr_b16 a[226:227], v13 offset:41984\n"
    "ds_read_b64_tr_b16 a[28:29], v137 offset:4096\n"
    "ds_read_b64_tr_b16 a[30:31], v137 offset:5120\n"
    "s_waitcnt lgkmcnt(8)\n"
    "v_mfma_f32_32x32x16_bf16 v[96:111], v[112:115], a[248:251], v[96:111]\n"
    "ds_read_b64_tr_b16 a[228:229], v13 offset:45056\n"
    "ds_read_b64_tr_b16 a[230:231], v13 offset:46080\n"
    "s_waitcnt lgkmcnt(8)\n"
    "v_mfma_f32_32x32x16_bf16 v[96:111], v[112:115], a[252:255], v[96:111]\n"
    "ds_read_b64_tr_b16 a[232:233], v13 offset:49152\n"
    "ds_read_b64_tr_b16 a[234:235], v13 offset:50176\n"
    "s_waitcnt lgkmcnt(4)\n"
    "s_mul_i32 s5, s88, 14\n"
    "s_add_u32 m0, s75, 4096\n"
    "s_nop 0\n"
    "buffer_load_dwordx4 v4, s[36:39], s5 offen lds\n"
    "v_mfma_f32_32x32x16_bf16 v[96:111], v[112:115], a[224:227], v[96:111]\n"
    "ds_read_b64_tr_b16 a[236:237], v13 offset:53248\n"
    "ds_read_b64_tr_b16 a[238:239], v13 offset:54272\n"
    "buffer_load_dwordx4 v143, s[36:39], s5 offen offset:1024 lds\n"
    "s_waitcnt lgkmcnt(4)\n"
    "v_mfma_f32_32x32x16_bf16 v[96:111], v[112:115], a[228:231], v[96:111]\n"
    "s_waitcnt lgkmcnt(2)\n"
    "v_mfma_f32_32x32x16_bf16 v[96:111], v[112:115], a[232:235], v[96:111]\n"
    "s_waitcnt lgkmcnt(0)\n"
    "v_mfma_f32_32x32x16_bf16 v[96:111], v[112:115], a[236:239], v[96:111]\n"
    "s_waitcnt vmcnt(9)\n"
    "ds_read_b64_tr_b16 a[32:33], v136 offset:6144\n"
    "ds_read_b64_tr_b16 a[34:35], v136 offset:7168\n"
    "ds_read_b64_tr_b16 a[36:37], v137 offset:6144\n"
    "ds_read_b64_tr_b16 a[38:39], v137 offset:7168\n"
    "s_waitcnt lgkmcnt(0)\n"
    "s_add_u32 s5, s5, 14336\n"
    "s_add_u32 m0, s75, 6144\n"
    "s_nop 0\n"
    "buffer_load_dwordx4 v4, s[36:39], s5 offen lds\n"
    "buffer_load_dwordx4 v143, s[36:39], s5 offen offset:1024 lds\n"
    "s_waitcnt vmcnt(8)\n"
    "ds_read_b64_tr_b16 a[40:41], v136 offset:8192\n"
    "ds_read_b64_tr_b16 a[42:43], v136 offset:9216\n"
    "ds_read_b64_tr_b16 a[44:45], v137 offset:8192\n"
    "ds_read_b64_tr_b16 a[46:47], v137 offset:9216\n"
    "s_waitcnt lgkmcnt(0)\n"
    "s_mul_i32 s5, s89, 14\n"
    "s_add_u32 m0, s75, 8192\n"
    "s_nop 0\n"
    "buffer_load_dwordx4 v4, s[36:39], s5 offen lds\n"
    "buffer_load_dwordx4 v143, s[36:39], s5 offen offset:1024 lds\n"
    "s_waitcnt vmcnt(8)\n"
    "ds_read_b64_tr_b16 a[48:49], v136 offset:0\n"
    "ds_read_b64_tr_b16 a[50:51], v136 offset:1024\n"
    "ds_read_b64_tr_b16 a[52:53], v137 offset:0\n"
    "ds_read_b64_tr_b16 a[54:55], v137 offset:1024\n"
    "s_waitcnt lgkmcnt(0)\n"
    "s_add_u32 s5, s5, 14336\n"
    "s_add_u32 m0, s75, 0\n"
    "s_nop 0\n"
    "buffer_load_dwordx4 v4, s[36:39], s5 offen lds\n"
    "buffer_load_dwordx4 v143, s[36:39], s5 offen offset:1024 lds\n"
    "s_waitcnt vmcnt(8)\n"
    "ds_read_b64_tr_b16 a[56:57], v136 offset:2048\n"
    "ds_read_b64_tr_b16 a[58:59], v136 offset:3072\n"
    "ds_read_b64_tr_b16 a[60:61], v137 offset:2048\n"
    "ds_read_b64_tr_b16 a[62:63], v137 offset:3072\n"
    "s_waitcnt lgkmcnt(0)\n"
    "s_mul_i32 s5, s90, 14\n"
    "s_add_u32 m0, s75, 2048\n"
    "s_nop 0\n"
    "buffer_load_dwordx4 v4, s[36:39], s5 offen lds\n"
    "buffer_load_dwordx4 v143, s[36:39], s5 offen offset:1024 lds\n"
    "s_waitcnt vmcnt(8)\n"
    "ds_read_b64_tr_b16 a[64:65], v136 offset:4096\n"
    "ds_read_b64_tr_b16 a[66:67], v136 offset:5120\n"
    "ds_read_b64_tr_b16 a[68:69], v137 offset:4096\n"
    "ds_read_b64_tr_b16 a[70:71], v137 offset:5120\n"
    "s_waitcnt lgkmcnt(0)\n"
    "s_add_u32 s5, s5, 14336\n"
    "s_add_u32 m0, s75, 4096\n"
    "s_nop 0\n"
    "buffer_load_dwordx4 v4, s[36:39], s5 offen lds\n"
    "buffer_load_dwordx4 v143, s[36:39], s5 offen offset:1024 lds\n"
    "s_waitcnt vmcnt(8)\n"
    "ds_read_b64_tr_b16 a[72:73], v136 offset:6144\n"
    "ds_read_b64_tr_b16 a[74:75], v136 offset:7168\n"
    "ds_read_b64_tr_b16 a[76:77], v137 offset:6144\n"
    "ds_read_b64_tr_b16 a[78:79], v137 offset:7168\n"
    "s_waitcnt lgkmcnt(0)\n"
    "s_mul_i32 s5, s84, 14\n"
    "s_add_u32 s5, s5, 28672\n"
    "s_add_u32 m0, s75, 6144\n"
    "s_nop 0\n"
    "buffer_load_dwordx4 v4, s[36:39], s5 offen lds\n"
    "buffer_load_dwordx4 v143, s[36:39], s5 offen offset:1024 lds\n"
    "s_mul_i32 s60, s51, 229376\n"
    "s_mul_i32 s71, s48, 28672\n"
    "s_add_u32 s60, s60, s71\n"
    "s_add_u32 s60, s60, 6291456\n"
    "v_add_u32 v141, s60, v1\n"
    "v_add_u32 v142, 114688, v141\n"
    "v_lshrrev_b32 v24, 1, v22\n"
    "v_min_u32 v24, 6, v24\n"
    "v_lshlrev_b32 v24, 12, v24\n"
    "v_and_b32 v28, 1, v22\n"
    "v_lshl_add_u32 v12, v28, 10, v24\n"
    "s_add_u32 s66, s60, 114688\n"
    "s_mul_i32 s67, s51, 57344\n"
    "s_add_u32 s67, s67, 4194304\n"
    "s_lshl_b32 s71, s52, 2\n"
    "s_add_u32 s71, s71, s48\n"
    "s_lshl_b32 s71, s71, 10\n"
    "s_add_u32 s67, s67, s71\n"
    "s_add_u32 s68, s67, 28672\n"
    "s_lshl_b32 s72, s74, 5\n"
    "s_add_u32 s72, s72, s54\n"
    "s_lshl_b32 s72, s72, 7\n"
    "s_add_u32 s72, s72, 131072\n"
    "s_lshl_b32 s65, s51, 5\n"
    "s_mul_i32 s71, s48, 7\n"
    "s_add_u32 s65, s65, s71\n"
    "s_lshl_b32 s65, s65, 7\n"
    "s_add_u32 s65, s65, 131072\n"
    "v_min_u32 v13, 6, v22\n"
    "v_lshlrev_b32 v13, 7, v13\n"
    "v_cmp_eq_u32 vcc, 0, v22\n"
    "v_mov_b32 v2, 2147483392\n"
    "s_nop 0\n"
    "v_cndmask_b32 v2, v2, v22, vcc\n"
    "v_mul_u32_u24 v15, 896, v26\n"
    "v_lshl_add_u32 v15, v27, 4, v15\n"
    "s_mul_i32 s69, s51, 57344\n"
    "s_lshl_b32 s71, s52, 7\n"
    "s_add_u32 s69, s69, s71\n"
    "s_lshl_b32 s71, s48, 5\n"
    "s_add_u32 s69, s69, s71\n"
    "s_add_u32 s70, s69, 28672\n"
    "v_cvt_pk_bf16_f32 v24, v96, v96\n"
    "v_lshlrev_b32 v24, 16, v24\n"
    "v_sub_f32 v24, v96, v24\n"
    "v_cvt_pk_bf16_f32 v24, v96, v24\n"
    "v_lshlrev_b32 v138, 2, v22\n"
    "v_add_u32 v138, 159744, v138\n"
    "s_lshl_b32 s71, s48, 8\n"
    "v_add_u32 v28, s71, v138\n"
    "ds_write_b32 v28, v24\n"
    "ds_read_b64_tr_b16 a[224:225], v8 offset:0\n"
    "ds_read_b64_tr_b16 a[226:227], v8 offset:1024\n"
    "ds_read_b64_tr_b16 a[228:229], v9 offset:0\n"
    "ds_read_b64_tr_b16 a[230:231], v9 offset:1024\n"
    "ds_read_b64_tr_b16 a[232:233], v8 offset:4096\n"
    "ds_read_b64_tr_b16 a[234:235], v8 offset:5120\n"
    "ds_read_b64_tr_b16 a[236:237], v9 offset:4096\n"
    "ds_read_b64_tr_b16 a[238:239], v9 offset:5120\n"
    "ds_read_b64_tr_b16 a[240:241], v8 offset:8192\n"
    "ds_read_b64_tr_b16 a[242:243], v8 offset:9216\n"
    "ds_read_b64_tr_b16 a[244:245], v9 offset:8192\n"
    "ds_read_b64_tr_b16 a[246:247], v9 offset:9216\n"
    "buffer_load_dword v31, v14, s[44:47], s73 offen sc1\n"
    "s_waitcnt vmcnt(0)\n"
    "v_cmp_ne_u32 vcc, s56, v31\n"
    "s_cbranch_vccnz L_retry_17_%=\n"
    "L_go_18_%=:\n"
    "buffer_load_dwordx4 v[144:147], v139, s[44:47], s84 offen offset:0 sc1\n"
    "buffer_load_dwordx4 v[148:151], v139, s[44:47], s84 offen offset:1024 sc1\n"
    "buffer_load_dwordx4 v[152:155], v139, s[44:47], s84 offen offset:2048 sc1\n"
    "buffer_load_dwordx4 v[156:159], v139, s[44:47], s84 offen offset:3072 sc1\n"
    "buffer_load_dwordx4 v[160:163], v139, s[44:47], s85 offen offset:0 sc1\n"
    "buffer_load_dwordx4 v[164:167], v139, s[44:47], s85 offen offset:1024 sc1\n"
    "buffer_load_dwordx4 v[168:171], v139, s[44:47], s85 offen offset:2048 sc1\n"
    "buffer_load_dwordx4 v[172:175], v139, s[44:47], s85 offen offset:3072 sc1\n"
    "buffer_load_dwordx4 v[176:179], v139, s[44:47], s86 offen offset:0 sc1\n"
    "buffer_load_dwordx4 v[180:183], v139, s[44:47], s86 offen offset:1024 sc1\n"
    "s_waitcnt vmcnt(9) lgkmcnt(8)\n"
    "v_mfma_f32_32x32x16_bf16 v[32:47], a[224:227], v[144:147], 0\n"
    "ds_read_b64_tr_b16 a[248:249], v8 offset:12288\n"
    "ds_read_b64_tr_b16 a[250:251], v8 offset:13312\n"
    "buffer_load_dwordx4 v[184:187], v139, s[44:47], s86 offen offset:2048 sc1\n"
    "v_mfma_f32_32x32x16_bf16 v[48:63], a[228:231], v[144:147], 0\n"
    "ds_read_b64_tr_b16 a[252:253], v9 offset:12288\n"
    "ds_read_b64_tr_b16 a[254:255], v9 offset:13312\n"
    "s_waitcnt vmcnt(9) lgkmcnt(8)\n"
    "v_mfma_f32_32x32x16_bf16 v[32:47], a[232:235], v[148:151], v[32:47]\n"
    "ds_read_b64_tr_b16 a[224:225], v8 offset:16384\n"
    "ds_read_b64_tr_b16 a[226:227], v8 offset:17408\n"
    "buffer_load_dwordx4 v[188:191], v139, s[44:47], s86 offen offset:3072 sc1\n"
    "v_mfma_f32_32x32x16_bf16 v[48:63], a[236:239], v[148:151], v[48:63]\n"
    "ds_read_b64_tr_b16 a[228:229], v9 offset:16384\n"
    "ds_read_b64_tr_b16 a[230:231], v9 offset:17408\n"
    "s_waitcnt vmcnt(9) lgkmcnt(8)\n"
    "v_mfma_f32_32x32x16_bf16 v[32:47], a[240:243], v[152:155], v[32:47]\n"
    "ds_read_b64_tr_b16 a[232:233], v8 offset:20480\n"
    "ds_read_b64_tr_b16 a[234:235], v8 offset:21504\n"
    "buffer_load_dwordx4 v[192:195], v139, s[44:47], s87 offen offset:0 sc1\n"
    "v_mfma_f32_32x32x16_bf16 v[48:63], a[244:247], v[152:155], v[48:63]\n"
    "ds_read_b64_tr_b16 a[236:237], v9 offset:20480\n"
    "ds_read_b64_tr_b16 a[238:239], v9 offset:21504\n"
    "s_waitcnt vmcnt(9) lgkmcnt(8)\n"
    "v_mfma_f32_32x32x16_bf16 v[32:47], a[248:251], v[156:159], v[32:47]\n"
    "ds_read_b64_tr_b16 a[240:241], v8 offset:24576\n"
    "ds_read_b64_tr_b16 a[242:243], v8 offset:25600\n"
    "buffer_load_dwordx4 v[196:199], v139, s[44:47], s87 offen offset:1024 sc1\n"
    "v_mfma_f32_32x32x16_bf16 v[48:63], a[252:255], v[156:159], v[48:63]\n"
    "ds_read_b64_tr_b16 a[244:245], v9 offset:24576\n"
    "ds_read_b64_tr_b16 a[246:247], v9 offset:25600\n"
    "s_waitcnt vmcnt(9) lgkmcnt(8)\n"
    "v_mfma_f32_32x32x16_bf16 v[32:47], a[224:227], v[160:163], v[32:47]\n"
    "ds_read_b64_tr_b16 a[248:249], v8 offset:28672\n"
    "ds_read_b64_tr_b16 a[250:251], v8 offset:29696\n"
    "buffer_load_dwordx4 v[200:203], v139, s[44:47], s87 offen offset:2048 sc1\n"
    "v_mfma_f32_32x32x16_bf16 v[48:63], a[228:231], v[160:163], v[48:63]\n"
    "ds_read_b64_tr_b16 a[252:253], v9 offset:28672\n"
    "ds_read_b64_tr_b16 a[254:255], v9 offset:29696\n"
    "s_waitcnt vmcnt(9) lgkmcnt(8)\n"
    "v_mfma_f32_32x32x16_bf16 v[32:47], a[232:235], v[164:167], v[32:47]\n"
    "ds_read_b64_tr_b16 a[224:225], v8 offset:32768\n"
    "ds_read_b64_tr_b16 a[226:227], v8 offset:33792\n"
    "buffer_load_dwordx4 v[204:207], v139, s[44:47], s87 offen offset:3072 sc1\n"
    "v_mfma_f32_32x32x16_bf16 v[48:63], a[236:239], v[164:167], v[48:63]\n"
    "ds_read_b64_tr_b16 a[228:229], v9 offset:32768\n"
    "ds_read_b64_tr_b16 a[230:231], v9 offset:33792\n"
    "s_waitcnt vmcnt(9) lgkmcnt(8)\n"
    "v_mfma_f32_32x32x16_bf16 v[32:47], a[240:243], v[168:171], v[32:47]\n"
    "ds_read_b64_tr_b16 a[232:233], v8 offset:36864\n"
    "ds_read_b64_tr_b16 a[234:235], v8 offset:37888\n"
    "buffer_load_dwordx4 v[208:211], v139, s[44:47], s88 offen offset:0 sc1\n"
    "v_mfma_f32_32x32x16_bf16 v[48:63], a[244:247], v[168:171], v[48:63]\n"
    "ds_read_b64_tr_b16 a[236:237], v9 offset:36864\n"
    "ds_read_b64_tr_b16 a[238:239], v9 offset:37888\n"
    "s_waitcnt vmcnt(9) lgkmcnt(8)\n"
    "v_mfma_f32_32x32x16_bf16 v[32:47], a[248:251], v[172:175], v[32:47]\n"
    "ds_read_b64_tr_b16 a[240:241], v8 offset:40960\n"
    "ds_read_b64_tr_b16 a[242:243], v8 offset:41984\n"
    "buffer_load_dwordx4 v[212:215], v139, s[44:47], s88 offen offset:1024 sc1\n"
    "v_mfma_f32_32x32x16_bf16 v[48:63], a[252:255], v[172:175], v[48:63]\n"
    "ds_read_b64_tr_b16 a[244:245], v9 offset:40960\n"
    "ds_read_b64_tr_b16 a[246:247], v9 offset:41984\n"
    "s_waitcnt vmcnt(9) lgkmcnt(8)\n"
    "v_mfma_f32_32x32x16_bf16 v[32:47], a[224:227], v[176:179], v[32:47]\n"
    "ds_read_b64_tr_b16 a[248:249], v8 offset:45056\n"
    "ds_read_b64_tr_b16 a[250:251], v8 offset:46080\n"
    "buffer_load_dwordx4 v[216:219], v139, s[44:47], s88 offen offset:2048 sc1\n"
    "v_mfma_f32_32x32x16_bf16 v[48:63], a[228:231], v[176:179], v[48:63]\n"
    "ds_read_b64_tr_b16 a[252:253], v9 offset:45056\n"
    "ds_read_b64_tr_b16 a[254:255], v9 offset:46080\n"
    "s_waitcnt vmcnt(9) lgkmcnt(8)\n"
    "v_mfma_f32_32x32x16_bf16 v[32:47], a[232:235], v[180:183], v[32:47]\n"
    "ds_read_b64_tr_b16 a[224:225], v8 offset:49152\n"
    "ds_read_b64_tr_b16 a[226:227], v8 offset:50176\n"
    "buffer_load_dwordx4 v[220:223], v139, s[44:47], s88 offen offset:3072 sc1\n"
    "v_mfma_f32_32x32x16_bf16 v[48:63], a[236:239], v[180:183], v[48:63]\n"
    "ds_read_b64_tr_b16 a[228:229], v9 offset:49152\n"
    "ds_read_b64_tr_b16 a[230:231], v9 offset:50176\n"
    "s_waitcnt vmcnt(9) lgkmcnt(8)\n"
    "v_mfma_f32_32x32x16_bf16 v[32:47], a[240:243], v[184:187], v[32:47]\n"
    "ds_read_b64_tr_b16 a[232:233], v8 offset:53248\n"
    "ds_read_b64_tr_b16 a[234:235], v8 offset:54272\n"
    "buffer_load_dwordx4 v[224:227], v139, s[44:47], s89 offen offset:0 sc1\n"
    "v_mfma_f32_32x32x16_bf16 v[48:63], a[244:247], v[184:187], v[48:63]\n"
    "ds_read_b64_tr_b16 a[236:237], v9 offset:53248\n"
    "ds_read_b64_tr_b16 a[238:239], v9 offset:54272\n"
    "s_waitcnt vmcnt(9) lgkmcnt(8)\n"
    "v_mfma_f32_32x32x16_bf16 v[32:47], a[248:251], v[188:191], v[32:47]\n"
    "ds_read_b64_tr_b16 a[240:241], v10 offset:0\n"
    "ds_read_b64_tr_b16 a[242:243], v10 offset:1024\n"
    "buffer_load_dwordx4 v[228:231], v139, s[44:47], s89 offen offset:1024 sc1\n"
    "v_mfma_f32_32x32x16_bf16 v[48:63], a[252:255], v[188:191], v[48:63]\n"
    "ds_read_b64_tr_b16 a[244:245], v11 offset:0\n"
    "ds_read_b64_tr_b16 a[246:247], v11 offset:1024\n"
    "s_waitcnt vmcnt(9) lgkmcnt(8)\n"
    "v_mfma_f32_32x32x16_bf16 v[32:47], a[224:227], v[192:195], v[32:47]\n"
    "ds_read_b64_tr_b16 a[248:249], v10 offset:4096\n"
    "ds_read_b64_tr_b16 a[250:251], v10 offset:5120\n"
    "buffer_load_dwordx4 v[232:235], v139, s[44:47], s89 offen offset:2048 sc1\n"
    "v_mfma_f32_32x32x16_bf16 v[48:63], a[228:231], v[192:195], v[48:63]\n"
    "ds_read_b64_tr_b16 a[252:253], v11 offset:4096\n"
    "ds_read_b64_tr_b16 a[254:255], v11 offset:5120\n"
    "s_waitcnt vmcnt(9) lgkmcnt(8)\n"
    "v_mfma_f32_32x32x16_bf16 v[32:47], a[232:235], v[196:199], v[32:47]\n"
    "ds_read_b64_tr_b16 a[224:225], v10 offset:8192\n"
    "ds_read_b64_tr_b16 a[226:227], v10 offset:9216\n"
    "buffer_load_dwordx4 v[236:239], v139, s[44:47], s89 offen offset:3072 sc1\n"
    "v_mfma_f32_32x32x16_bf16 v[48:63], a[236:239], v[196:199], v[48:63]\n"
    "ds_read_b64_tr_b16 a[228:229], v11 offset:8192\n"
    "ds_read_b64_tr_b16 a[230:231], v11 offset:9216\n"
    "s_waitcnt vmcnt(9) lgkmcnt(8)\n"
    "v_mfma_f32_32x32x16_bf16 v[32:47], a[240:243], v[200:203], v[32:47]\n"
    "ds_read_b64_tr_b16 a[232:233], v10 offset:12288\n"
    "ds_read_b64_tr_b16 a[234:235], v10 offset:13312\n"
    "buffer_load_dwordx4 v[240:243], v139, s[44:47], s90 offen offset:0 sc1\n"
    "v_mfma_f32_32x32x16_bf16 v[48:63], a[244:247], v[200:203], v[48:63]\n"
    "ds_read_b64_tr_b16 a[236:237], v11 offset:12288\n"
    "ds_read_b64_tr_b16 a[238:239], v11 offset:13312\n"
    "s_waitcnt vmcnt(9) lgkmcnt(8)\n"
    "v_mfma_f32_32x32x16_bf16 v[32:47], a[248:251], v[204:207], v[32:47]\n"
    "ds_read_b64_tr_b16 a[240:241], v10 offset:16384\n"
    "ds_read_b64_tr_b16 a[242:243], v10 offset:17408\n"
    "buffer_load_dwordx4 v[244:247], v139, s[44:47], s90 offen offset:1024 sc1\n"
    "s_barrier\n"
    "ds_read2_b32 v[96:97], v138 offset0:0 offset1:0\n"
    "ds_read2_b32 v[98:99], v138 offset0:64 offset1:64\n"
    "ds_read2_b32 v[100:101], v138 offset0:128 offset1:128\n"
    "ds_read2_b32 v[102:103], v138 offset0:192 offset1:192\n"
    "v_mfma_f32_32x32x16_bf16 v[48:63], a[252:255], v[204:207], v[48:63]\n"
    "ds_read_b64_tr_b16 a[244:245], v11 offset:16384\n"
    "ds_read_b64_tr_b16 a[246:247], v11 offset:17408\n"
    "s_waitcnt vmcnt(9) lgkmcnt(12)\n"
    "v_mfma_f32_32x32x16_bf16 v[32:47], a[224:227], v[208:211], v[32:47]\n"
    "ds_read_b64_tr_b16 a[248:249], v10 offset:20480\n"
    "ds_read_b64_tr_b16 a[250:251], v10 offset:21504\n"
    "buffer_load_dwordx4 v[248:251], v139, s[44:47], s90 offen offset:2048 sc1\n"
    "v_mfma_f32_32x32x16_bf16 v[48:63], a[228:231], v[208:211], v[48:63]\n"
    "ds_read_b64_tr_b16 a[252:253], v11 offset:20480\n"
    "ds_read_b64_tr_b16 a[254:255], v11 offset:21504\n"
    "s_waitcnt vmcnt(9) lgkmcnt(12)\n"
    "v_mfma_f32_32x32x16_bf16 v[32:47], a[232:235], v[212:215], v[32:47]\n"
    "ds_read_b64_tr_b16 a[224:225], v10 offset:24576\n"
    "ds_read_b64_tr_b16 a[226:227], v10 offset:25600\n"
    "buffer_load_dwordx4 v[252:255], v139, s[44:47], s90 offen offset:3072 sc1\n"
    "v_mfma_f32_32x32x16_bf16 v[48:63], a[236:239], v[212:215], v[48:63]\n"
    "ds_read_b64_tr_b16 a[228:229], v11 offset:24576\n"
    "ds_read_b64_tr_b16 a[230:231], v11 offset:25600\n"
    "s_waitcnt vmcnt(9) lgkmcnt(8)\n"
    "v_mfma_f32_32x32x16_bf16 v[32:47], a[240:243], v[216:219], v[32:47]\n"
    "ds_read_b64_tr_b16 a[232:233], v10 offset:28672\n"
    "ds_read_b64_tr_b16 a[234:235], v10 offset:29696\n"
    "buffer_load_dwordx4 v[120:123], v1, s[44:47], s61 offen offset:0 sc1\n"
    "v_mfma_f32_32x32x16_bf16 v[48:63], a[244:247], v[216:219], v[48:63]\n"
    "ds_read_b64_tr_b16 a[236:237], v11 offset:28672\n"
    "ds_read_b64_tr_b16 a[238:239], v11 offset:29696\n"
    "s_waitcnt vmcnt(9) lgkmcnt(8)\n"
    "v_mfma_f32_32x32x16_bf16 v[32:47], a[248:251], v[220:223], v[32:47]\n"
    "ds_read_b64_tr_b16 a[240:241], v10 offset:32768\n"
    "ds_read_b64_tr_b16 a[242:243], v10 offset:33792\n"
    "buffer_load_dwordx4 v[124:127], v1, s[44:47], s61 offen offset:1024 sc1\n"
    "ds_read_b64_tr_b16 a[80:81], v136 offset:8192\n"
    "ds_read_b64_tr_b16 a[82:83], v136 offset:9216\n"
    "v_mfma_f32_32x32x16_bf16 v[48:63], a[252:255], v[220:223], v[48:63]\n"
    "ds_read_b64_tr_b16 a[244:245], v11 offset:32768\n"
    "ds_read_b64_tr_b16 a[246:247], v11 offset:33792\n"
    "ds_read_b64_tr_b16 a[84:85], v137 offset:8192\n"
    "ds_read_b64_tr_b16 a[86:87], v137 offset:9216\n"
    "s_waitcnt vmcnt(9) lgkmcnt(12)\n"
    "v_mfma_f32_32x32x16_bf16 v[32:47], a[224:227], v[224:227], v[32:47]\n"
    "ds_read_b64_tr_b16 a[248:249], v10 offset:36864\n"
    "ds_read_b64_tr_b16 a[250:251], v10 offset:37888\n"
    "buffer_load_dwordx4 v[128:131], v1, s[44:47], s61 offen offset:2048 sc1\n"
    "v_mfma_f32_32x32x16_bf16 v[48:63], a[228:231], v[224:227], v[48:63]\n"
    "ds_read_b64_tr_b16 a[252:253], v11 offset:36864\n"
    "ds_read_b64_tr_b16 a[254:255], v11 offset:37888\n"
    "ds_read_b64_tr_b16 a[88:89], v136 offset:0\n"
    "ds_read_b64_tr_b16 a[90:91], v136 offset:1024\n"
    "s_waitcnt vmcnt(9) lgkmcnt(14)\n"
    "v_mfma_f32_32x32x16_bf16 v[32:47], a[232:235], v[228:231], v[32:47]\n"
    "ds_read_b64_tr_b16 a[224:225], v10 offset:40960\n"
    "ds_read_b64_tr_b16 a[226:227], v10 offset:41984\n"
    "buffer_load_dwordx4 v[132:135], v1, s[44:47], s61 offen offset:3072 sc1\n"
    "ds_read_b64_tr_b16 a[92:93], v137 offset:0\n"
    "ds_read_b64_tr_b16 a[94:95], v137 offset:1024\n"
    "v_mfma_f32_32x32x16_bf16 v[48:63], a[236:239], v[228:231], v[48:63]\n"
    "ds_read_b64_tr_b16 a[228:229], v11 offset:40960\n"
    "ds_read_b64_tr_b16 a[230:231], v11 offset:41984\n"
    "s_waitcnt vmcnt(9) lgkmcnt(14)\n"
    "v_mfma_f32_32x32x16_bf16 v[32:47], a[240:243], v[232:235], v[32:47]\n"
    "ds_read_b64_tr_b16 a[232:233], v10 offset:45056\n"
    "ds_read_b64_tr_b16 a[234:235], v10 offset:46080\n"
    "s_waitcnt lgkmcnt(14)\n"
    "s_add_u32 s5, s5, 14336\n"
    "s_add_u32 m0, s75, 8192\n"
    "s_nop 0\n"
    "buffer_load_dwordx4 v4, s[36:39], s5 offen lds\n"
    "v_mfma_f32_32x32x16_bf16 v[48:63], a[244:247], v[232:235], v[48:63]\n"
    "ds_read_b64_tr_b16 a[236:237], v11 offset:45056\n"
    "ds_read_b64_tr_b16 a[238:239], v11 offset:46080\n"
    "ds_read_b64_tr_b16 a[96:97], v136 offset:2048\n"
    "ds_read_b64_tr_b16 a[98:99], v136 offset:3072\n"
    "s_waitcnt vmcnt(9) lgkmcnt(14)\n"
    "v_mfma_f32_32x32x16_bf16 v[32:47], a[248:251], v[236:239], v[32:47]\n"
    "ds_read_b64_tr_b16 a[240:241], v10 offset:49152\n"
    "ds_read_b64_tr_b16 a[242:243], v10 offset:50176\n"
    "buffer_load_dwordx4 v143, s[36:39], s5 offen offset:1024 lds\n"
    "ds_read_b64_tr_b16 a[100:101], v137 offset:2048\n"
    "ds_read_b64_tr_b16 a[102:103], v137 offset:3072\n"
    "v_mfma_f32_32x32x16_bf16 v[48:63], a[252:255], v[236:239], v[48:63]\n"
    "ds_read_b64_tr_b16 a[244:245], v11 offset:49152\n"
    "ds_read_b64_tr_b16 a[246:247], v11 offset:50176\n"
    "s_waitcnt vmcnt(9) lgkmcnt(12)\n"
    "v_mfma_f32_32x32x16_bf16 v[32:47], a[224:227], v[240:243], v[32:47]\n"
    "ds_read_b64_tr_b16 a[248:249], v10 offset:53248\n"
    "ds_read_b64_tr_b16 a[250:251], v10 offset:54272\n"
    "s_mul_i32 s5, s85, 14\n"
    "s_add_u32 s5, s5, 28672\n"
    "s_add_u32 m0, s75, 0\n"
    "s_nop 0\n"
    "buffer_load_dwordx4 v4, s[36:39], s5 offen lds\n"
    "v_mfma_f32_32x32x16_bf16 v[48:63], a[228:231], v[240:243], v[48:63]\n"
    "ds_read_b64_tr_b16 a[252:253], v11 offset:53248\n"
    "ds_read_b64_tr_b16 a[254:255], v11 offset:54272\n"
    "ds_read_b64_tr_b16 a[104:105], v136 offset:4096\n"
    "ds_read_b64_tr_b16 a[106:107], v136 offset:5120\n"
    "s_waitcnt vmcnt(9) lgkmcnt(14)\n"
    "v_mfma_f32_32x32x16_bf16 v[32:47], a[232:235], v[244:247], v[32:47]\n"
    "v_xor_b32 v8, 64, v8\n"
    "v_xor_b32 v9, 64, v9\n"
    "ds_read_b64_tr_b16 a[224:225], v8 offset:0\n"
    "ds_read_b64_tr_b16 a[226:227], v8 offset:1024\n"
    "buffer_load_dwordx4 v143, s[36:39], s5 offen offset:1024 lds\n"
    "ds_read_b64_tr_b16 a[108:109], v137 offset:4096\n"
    "v_mfma_f32_32x32x16_bf16 v[48:63], a[236:239], v[244:247], v[48:63]\n"
    "ds_read_b64_tr_b16 a[228:229], v9 offset:0\n"
    "ds_read_b64_tr_b16 a[230:231], v9 offset:1024\n"
    "ds_read_b64_tr_b16 a[110:111], v137 offset:5120\n"
    "s_waitcnt vmcnt(9) lgkmcnt(12)\n"
    "v_mfma_f32_32x32x16_bf16 v[32:47], a[240:243], v[248:251], v[32:47]\n"
    "ds_read_b64_tr_b16 a[232:233], v8 offset:4096\n"
    "ds_read_b64_tr_b16 a[234:235], v8 offset:5120\n"
    "s_add_u32 s5, s5, 14336\n"
    "s_add_u32 m0, s75, 2048\n"
    "s_nop 0\n"
    "buffer_load_dwordx4 v4, s[36:39], s5 offen lds\n"
    "v_mfma_f32_32x32x16_bf16 v[48:63], a[244:247], v[248:251], v[48:63]\n"
    "ds_read_b64_tr_b16 a[236:237], v9 offset:4096\n"
    "ds_read_b64_tr_b16 a[238:239], v9 offset:5120\n"
    "s_waitcnt vmcnt(9) lgkmcnt(12)\n"
    "v_mfma_f32_32x32x16_bf16 v[32:47], a[248:251], v[252:255], v[32:47]\n"
    "ds_read_b64_tr_b16 a[240:241], v8 offset:8192\n"
    "ds_read_b64_tr_b16 a[242:243], v8 offset:9216\n"
    "buffer_load_dwordx4 v143, s[36:39], s5 offen offset:1024 lds\n"
    "v_mfma_f32_32x32x16_bf16 v[48:63], a[252:255], v[252:255], v[48:63]\n"
    "ds_read_b64_tr_b16 a[244:245], v9 offset:8192\n"
    "ds_read_b64_tr_b16 a[246:247], v9 offset:9216\n"
    "s_waitcnt lgkmcnt(9)\n"
    "v_mfma_f32_32x32x16_bf16 v[64:79], a[224:227], v[144:147], 0\n"
    "ds_read_b64_tr_b16 a[248:249], v8 offset:12288\n"
    "ds_read_b64_tr_b16 a[250:251], v8 offset:13312\n"
    "s_waitcnt vmcnt(9)\n"
    "v_add_f32 v18, v120, v121\n"
    "v_add_f32 v19, v122, v123\n"
    "s_waitcnt vmcnt(8)\n"
    "v_add_f32 v18, v18, v124\n"
    "v_mfma_f32_32x32x16_bf16 v[80:95], a[228:231], v[144:147], 0\n"
    "ds_read_b64_tr_b16 a[252:253], v9 offset:12288\n"
    "ds_read_b64_tr_b16 a[254:255], v9 offset:13312\n"
    "v_add_f32 v19, v19, v125\n"
    "v_add_f32 v18, v18, v126\n"
    "buffer_load_dwordx4 v[144:147], v140, s[44:47], s84 offen offset:0 sc1\n"
    "s_waitcnt lgkmcnt(8)\n"
    "v_mfma_f32_32x32x16_bf16 v[64:79], a[232:235], v[148:151], v[64:79]\n"
    "ds_read_b64_tr_b16 a[224:225], v8 offset:16384\n"
    "ds_read_b64_tr_b16 a[226:227], v8 offset:17408\n"
    "v_add_f32 v19, v19, v127\n"
    "s_waitcnt vmcnt(8)\n"
    "v_add_f32 v18, v18, v128\n"
    "v_add_f32 v19, v19, v129\n"
    "v_mfma_f32_32x32x16_bf16 v[80:95], a[236:239], v[148:151], v[80:95]\n"
    "ds_read_b64_tr_b16 a[228:229], v9 offset:16384\n"
    "ds_read_b64_tr_b16 a[230:231], v9 offset:17408\n"
    "v_add_f32 v18, v18, v130\n"
    "v_add_f32 v19, v19, v131\n"
    "buffer_load_dwordx4 v[148:151], v140, s[44:47], s84 offen offset:1024 sc1\n"
    "s_waitcnt lgkmcnt(8)\n"
    "v_mfma_f32_32x32x16_bf16 v[64:79], a[240:243], v[152:155], v[64:79]\n"
    "ds_read_b64_tr_b16 a[232:233], v8 offset:20480\n"
    "ds_read_b64_tr_b16 a[234:235], v8 offset:21504\n"
    "s_waitcnt vmcnt(8)\n"
    "v_add_f32 v18, v18, v132\n"
    "v_add_f32 v19, v19, v133\n"
    "v_add_f32 v18, v18, v19\n"
    "v_mfma_f32_32x32x16_bf16 v[80:95], a[244:247], v[152:155], v[80:95]\n"
    "ds_read_b64_tr_b16 a[236:237], v9 offset:20480\n"
    "ds_read_b64_tr_b16 a[238:239], v9 offset:21504\n"
    "v_mov_b32 v19, v18\n"
    "s_nop 1\n"
    "v_permlane32_swap_b32 v18, v19\n"
    "buffer_load_dwordx4 v[152:155], v140, s[44:47], s84 offen offset:2048 sc1\n"
    "s_waitcnt lgkmcnt(8)\n"
    "v_mfma_f32_32x32x16_bf16 v[64:79], a[248:251], v[156:159], v[64:79]\n"
    "ds_read_b64_tr_b16 a[240:241], v8 offset:24576\n"
    "ds_read_b64_tr_b16 a[242:243], v8 offset:25600\n"
    "v_mul_f32 v18, -0.002232142857142857, v18\n"
    "v_mul_f32 v19, 0.002232142857142857, v19\n"
    "v_fma_f32 v19, -v18, v18, v19\n"
    "v_mfma_f32_32x32x16_bf16 v[80:95], a[252:255], v[156:159], v[80:95]\n"
    "ds_read_b64_tr_b16 a[244:245], v9 offset:24576\n"
    "ds_read_b64_tr_b16 a[246:247], v9 offset:25600\n"
    "v_max_f32 v19, 0, v19\n"
    "v_add_f32 v19, 1e-05, v19\n"
    "buffer_load_dwordx4 v[156:159], v140, s[44:47], s84 offen offset:3072 sc1\n"
    "s_waitcnt lgkmcnt(8)\n"
    "v_mfma_f32_32x32x16_bf16 v[64:79], a[224:227], v[160:163], v[64:79]\n"
    "ds_read_b64_tr_b16 a[248:249], v8 offset:28672\n"
    "ds_read_b64_tr_b16 a[250:251], v8 offset:29696\n"
    "v_rcp_f32 v19, v19\n"
    "v_cndmask_b32 v18, 0, v18, s[78:79]\n"
    "v_mfma_f32_32x32x16_bf16 v[80:95], a[228:231], v[160:163], v[80:95]\n"
    "ds_read_b64_tr_b16 a[252:253], v9 offset:28672\n"
    "ds_read_b64_tr_b16 a[254:255], v9 offset:29696\n"
    "v_cvt_pk_bf16_f32 v104, v18, v18\n"
    "v_lshlrev_b32 v105, 16, v104\n"
    "buffer_load_dwordx4 v[160:163], v140, s[44:47], s85 offen offset:0 sc1\n"
    "s_waitcnt lgkmcnt(8)\n"
    "v_mfma_f32_32x32x16_bf16 v[64:79], a[232:235], v[164:167], v[64:79]\n"
    "ds_read_b64_tr_b16 a[224:225], v8 offset:32768\n"
    "ds_read_b64_tr_b16 a[226:227], v8 offset:33792\n"
    "v_sub_f32 v105, v18, v105\n"
    "v_cvt_pk_bf16_f32 v105, v105, 0\n"
    "v_mov_b32 v106, 0\n"
    "v_mfma_f32_32x32x16_bf16 v[80:95], a[236:239], v[164:167], v[80:95]\n"
    "ds_read_b64_tr_b16 a[228:229], v9 offset:32768\n"
    "ds_read_b64_tr_b16 a[230:231], v9 offset:33792\n"
    "v_mov_b32 v107, 0\n"
    "buffer_load_dwordx4 v[164:167], v140, s[44:47], s85 offen offset:1024 sc1\n"
    "s_waitcnt lgkmcnt(8)\n"
    "v_mfma_f32_32x32x16_bf16 v[64:79], a[240:243], v[168:171], v[64:79]\n"
    "ds_read_b64_tr_b16 a[232:233], v8 offset:36864\n"
    "ds_read_b64_tr_b16 a[234:235], v8 offset:37888\n"
    "v_mfma_f32_32x32x16_bf16 v[32:47], v[96:99], v[104:107], v[32:47]\n"
    "v_mfma_f32_32x32x16_bf16 v[80:95], a[244:247], v[168:171], v[80:95]\n"
    "ds_read_b64_tr_b16 a[236:237], v9 offset:36864\n"
    "ds_read_b64_tr_b16 a[238:239], v9 offset:37888\n"
    "v_mfma_f32_32x32x16_bf16 v[48:63], v[98:101], v[104:107], v[48:63]\n"
    "buffer_load_dwordx4 v[168:171], v140, s[44:47], s85 offen offset:2048 sc1\n"
    "s_waitcnt lgkmcnt(8)\n"
    "v_mfma_f32_32x32x16_bf16 v[64:79], a[248:251], v[172:175], v[64:79]\n"
    "ds_read_b64_tr_b16 a[240:241], v8 offset:40960\n"
    "ds_read_b64_tr_b16 a[242:243], v8 offset:41984\n"
    "v_mfma_f32_32x32x16_bf16 v[80:95], a[252:255], v[172:175], v[80:95]\n"
    "ds_read_b64_tr_b16 a[244:245], v9 offset:40960\n"
    "ds_read_b64_tr_b16 a[246:247], v9 offset:41984\n"
    "v_mul_f32 v32, v32, v19\n"
    "v_mul_f32 v32, v32, v40\n"
    "buffer_load_dwordx4 v[172:175], v140, s[44:47], s85 offen offset:3072 sc1\n"
    "s_waitcnt lgkmcnt(8)\n"
    "v_mfma_f32_32x32x16_bf16 v[64:79], a[224:227], v[176:179], v[64:79]\n"
    "ds_read_b64_tr_b16 a[248:249], v8 offset:45056\n"
    "ds_read_b64_tr_b16 a[250:251], v8 offset:46080\n"
    "v_mul_f32 v33, v33, v19\n"
    "v_mul_f32 v33, v33, v41\n"
    "v_cvt_pk_bf16_f32 v112, v32, v33\n"
    "v_mfma_f32_32x32x16_bf16 v[80:95], a[228:231], v[176:179], v[80:95]\n"
    "ds_read_b64_tr_b16 a[252:253], v9 offset:45056\n"
    "ds_read_b64_tr_b16 a[254:255], v9 offset:46080\n"
    "v_mul_f32 v34, v34, v19\n"
    "v_mul_f32 v34, v34, v42\n"
    "buffer_load_dwordx4 v[176:179], v140, s[44:47], s86 offen offset:0 sc1\n"
    "s_waitcnt lgkmcnt(8)\n"
    "v_mfma_f32_32x32x16_bf16 v[64:79], a[232:235], v[180:183], v[64:79]\n"
    "ds_read_b64_tr_b16 a[224:225], v8 offset:49152\n"
    "ds_read_b64_tr_b16 a[226:227], v8 offset:50176\n"
    "v_mul_f32 v35, v35, v19\n"
    "v_mul_f32 v35, v35, v43\n"
    "v_cvt_pk_bf16_f32 v113, v34, v35\n"
    "v_mfma_f32_32x32x16_bf16 v[80:95], a[236:239], v[180:183], v[80:95]\n"
    "ds_read_b64_tr_b16 a[228:229], v9 offset:49152\n"
    "ds_read_b64_tr_b16 a[230:231], v9 offset:50176\n"
    "v_mul_f32 v36, v36, v19\n"
    "v_mul_f32 v36, v36, v44\n"
    "buffer_load_dwordx4 v[180:183], v140, s[44:47], s86 offen offset:1024 sc1\n"
    "s_waitcnt lgkmcnt(8)\n"
    "v_mfma_f32_32x32x16_bf16 v[64:79], a[240:243], v[184:187], v[64:79]\n"
    "ds_read_b64_tr_b16 a[232:233], v8 offset:53248\n"
    "ds_read_b64_tr_b16 a[234:235], v8 offset:54272\n"
    "v_mul_f32 v37, v37, v19\n"
    "v_mul_f32 v37, v37, v45\n"
    "v_cvt_pk_bf16_f32 v114, v36, v37\n"
    "v_mfma_f32_32x32x16_bf16 v[80:95], a[244:247], v[184:187], v[80:95]\n"
    "ds_read_b64_tr_b16 a[236:237], v9 offset:53248\n"
    "ds_read_b64_tr_b16 a[238:239], v9 offset:54272\n"
    "v_mul_f32 v38, v38, v19\n"
    "v_mul_f32 v38, v38, v46\n"
    "buffer_load_dwordx4 v[184:187], v140, s[44:47], s86 offen offset:2048 sc1\n"
    "s_waitcnt lgkmcnt(8)\n"
    "v_mfma_f32_32x32x16_bf16 v[64:79], a[248:251], v[188:191], v[64:79]\n"
    "v_xor_b32 v10, 64, v10\n"
    "v_xor_b32 v11, 64, v11\n"
    "ds_read_b64_tr_b16 a[240:241], v10 offset:0\n"
    "ds_read_b64_tr_b16 a[242:243], v10 offset:1024\n"
    "v_mul_f32 v39, v39, v19\n"
    "v_mfma_f32_32x32x16_bf16 v[80:95], a[252:255], v[188:191], v[80:95]\n"
    "ds_read_b64_tr_b16 a[244:245], v11 offset:0\n"
    "ds_read_b64_tr_b16 a[246:247], v11 offset:1024\n"
    "v_mul_f32 v39, v39, v47\n"
    "v_cvt_pk_bf16_f32 v115, v38, v39\n"
    "buffer_load_dwordx4 v[188:191], v140, s[44:47], s86 offen offset:3072 sc1\n"
    "s_waitcnt lgkmcnt(8)\n"
    "v_mfma_f32_32x32x16_bf16 v[64:79], a[224:227], v[192:195], v[64:79]\n"
    "ds_read_b64_tr_b16 a[248:249], v10 offset:4096\n"
    "ds_read_b64_tr_b16 a[250:251], v10 offset:5120\n"
    "buffer_store_dwordx4 v[112:115], v1, s[44:47], s63 offen offset:0 sc1\n"
    "v_mul_f32 v48, v48, v19\n"
    "v_mfma_f32_32x32x16_bf16 v[80:95], a[228:231], v[192:195], v[80:95]\n"
    "ds_read_b64_tr_b16 a[252:253], v11 offset:4096\n"
    "ds_read_b64_tr_b16 a[254:255], v11 offset:5120\n"
    "v_mul_f32 v48, v48, v56\n"
    "v_mul_f32 v49, v49, v19\n"
    "s_waitcnt lgkmcnt(8)\n"
    "v_mfma_f32_32x32x16_bf16 v[64:79], a[232:235], v[196:199], v[64:79]\n"
    "ds_read_b64_tr_b16 a[224:225], v10 offset:8192\n"
    "ds_read_b64_tr_b16 a[226:227], v10 offset:9216\n"
    "v_mul_f32 v49, v49, v57\n"
    "v_cvt_pk_bf16_f32 v116, v48, v49\n"
    "v_mul_f32 v50, v50, v19\n"
    "v_mfma_f32_32x32x16_bf16 v[80:95], a[236:239], v[196:199], v[80:95]\n"
    "ds_read_b64_tr_b16 a[228:229], v11 offset:8192\n"
    "ds_read_b64_tr_b16 a[230:231], v11 offset:9216\n"
    "v_mul_f32 v50, v50, v58\n"
    "v_mul_f32 v51, v51, v19\n"
    "buffer_load_dwordx4 v[192:195], v140, s[44:47], s87 offen offset:0 sc1\n"
    "s_waitcnt lgkmcnt(8)\n"
    "v_mfma_f32_32x32x16_bf16 v[64:79], a[240:243], v[200:203], v[64:79]\n"
    "ds_read_b64_tr_b16 a[232:233], v10 offset:12288\n"
    "ds_read_b64_tr_b16 a[234:235], v10 offset:13312\n"
    "v_mul_f32 v51, v51, v59\n"
    "v_cvt_pk_bf16_f32 v117, v50, v51\n"
    "v_mul_f32 v52, v52, v19\n"
    "v_mfma_f32_32x32x16_bf16 v[80:95], a[244:247], v[200:203], v[80:95]\n"
    "ds_read_b64_tr_b16 a[236:237], v11 offset:12288\n"
    "ds_read_b64_tr_b16 a[238:239], v11 offset:13312\n"
    "v_mul_f32 v52, v52, v60\n"
    "v_mul_f32 v53, v53, v19\n"
    "buffer_load_dwordx4 v[196:199], v140, s[44:47], s87 offen offset:1024 sc1\n"
    "s_waitcnt lgkmcnt(8)\n"
    "v_mfma_f32_32x32x16_bf16 v[64:79], a[248:251], v[204:207], v[64:79]\n"
    "ds_read_b64_tr_b16 a[240:241], v10 offset:16384\n"
    "ds_read_b64_tr_b16 a[242:243], v10 offset:17408\n"
    "v_mul_f32 v53, v53, v61\n"
    "v_cvt_pk_bf16_f32 v118, v52, v53\n"
    "v_mul_f32 v54, v54, v19\n"
    "v_mfma_f32_32x32x16_bf16 v[80:95], a[252:255], v[204:207], v[80:95]\n"
    "ds_read_b64_tr_b16 a[244:245], v11 offset:16384\n"
    "ds_read_b64_tr_b16 a[246:247], v11 offset:17408\n"
    "v_mul_f32 v54, v54, v62\n"
    "v_mul_f32 v55, v55, v19\n"
    "buffer_load_dwordx4 v[200:203], v140, s[44:47], s87 offen offset:2048 sc1\n"
    "s_waitcnt lgkmcnt(8)\n"
    "v_mfma_f32_32x32x16_bf16 v[64:79], a[224:227], v[208:211], v[64:79]\n"
    "ds_read_b64_tr_b16 a[248:249], v10 offset:20480\n"
    "ds_read_b64_tr_b16 a[250:251], v10 offset:21504\n"
    "v_mul_f32 v55, v55, v63\n"
    "v_cvt_pk_bf16_f32 v119, v54, v55\n"
    "buffer_store_dwordx4 v[116:119], v1, s[44:47], s63 offen offset:1024 sc1\n"
    "v_mfma_f32_32x32x16_bf16 v[80:95], a[228:231], v[208:211], v[80:95]\n"
    "ds_read_b64_tr_b16 a[252:253], v11 offset:20480\n"
    "ds_read_b64_tr_b16 a[254:255], v11 offset:21504\n"
    "s_waitcnt lgkmcnt(8)\n"
    "v_mfma_f32_32x32x16_bf16 v[64:79], a[232:235], v[212:215], v[64:79]\n"
    "ds_read_b64_tr_b16 a[224:225], v10 offset:24576\n"
    "ds_read_b64_tr_b16 a[226:227], v10 offset:25600\n"
    "v_mfma_f32_32x32x16_bf16 v[80:95], a[236:239], v[212:215], v[80:95]\n"
    "ds_read_b64_tr_b16 a[228:229], v11 offset:24576\n"
    "ds_read_b64_tr_b16 a[230:231], v11 offset:25600\n"
    "buffer_load_dwordx4 v[204:207], v140, s[44:47], s87 offen offset:3072 sc1\n"
    "s_waitcnt lgkmcnt(8)\n"
    "v_mfma_f32_32x32x16_bf16 v[64:79], a[240:243], v[216:219], v[64:79]\n"
    "ds_read_b64_tr_b16 a[232:233], v10 offset:28672\n"
    "ds_read_b64_tr_b16 a[234:235], v10 offset:29696\n"
    "v_mfma_f32_32x32x16_bf16 v[80:95], a[244:247], v[216:219], v[80:95]\n"
    "ds_read_b64_tr_b16 a[236:237], v11 offset:28672\n"
    "ds_read_b64_tr_b16 a[238:239], v11 offset:29696\n"
    "buffer_load_dwordx4 v[208:211], v140, s[44:47], s88 offen offset:0 sc1\n"
    "s_waitcnt lgkmcnt(8)\n"
    "v_mfma_f32_32x32x16_bf16 v[64:79], a[248:251], v[220:223], v[64:79]\n"
    "ds_read_b64_tr_b16 a[240:241], v10 offset:32768\n"
    "ds_read_b64_tr_b16 a[242:243], v10 offset:33792\n"
    "v_mfma_f32_32x32x16_bf16 v[80:95], a[252:255], v[220:223], v[80:95]\n"
    "ds_read_b64_tr_b16 a[244:245], v11 offset:32768\n"
    "ds_read_b64_tr_b16 a[246:247], v11 offset:33792\n"
    "buffer_load_dwordx4 v[212:215], v140, s[44:47], s88 offen offset:1024 sc1\n"
    "s_waitcnt lgkmcnt(8)\n"
    "v_mfma_f32_32x32x16_bf16 v[64:79], a[224:227], v[224:227], v[64:79]\n"
    "ds_read_b64_tr_b16 a[248:249], v10 offset:36864\n"
    "ds_read_b64_tr_b16 a[250:251], v10 offset:37888\n"
    "v_mfma_f32_32x32x16_bf16 v[80:95], a[228:231], v[224:227], v[80:95]\n"
    "ds_read_b64_tr_b16 a[252:253], v11 offset:36864\n"
    "ds_read_b64_tr_b16 a[254:255], v11 offset:37888\n"
    "buffer_load_dwordx4 v[216:219], v140, s[44:47], s88 offen offset:2048 sc1\n"
    "s_waitcnt lgkmcnt(8)\n"
    "v_mfma_f32_32x32x16_bf16 v[64:79], a[232:235], v[228:231], v[64:79]\n"
    "ds_read_b64_tr_b16 a[224:225], v10 offset:40960\n"
    "ds_read_b64_tr_b16 a[226:227], v10 offset:41984\n"
    "v_mfma_f32_32x32x16_bf16 v[80:95], a[236:239], v[228:231], v[80:95]\n"
    "ds_read_b64_tr_b16 a[228:229], v11 offset:40960\n"
    "ds_read_b64_tr_b16 a[230:231], v11 offset:41984\n"
    "buffer_load_dwordx4 v[220:223], v140, s[44:47], s88 offen offset:3072 sc1\n"
    "s_waitcnt lgkmcnt(8)\n"
    "v_mfma_f32_32x32x16_bf16 v[64:79], a[240:243], v[232:235], v[64:79]\n"
    "ds_read_b64_tr_b16 a[232:233], v10 offset:45056\n"
    "ds_read_b64_tr_b16 a[234:235], v10 offset:46080\n"
    "v_mfma_f32_32x32x16_bf16 v[80:95], a[244:247], v[232:235], v[80:95]\n"
    "ds_read_b64_tr_b16 a[236:237], v11 offset:45056\n"
    "ds_read_b64_tr_b16 a[238:239], v11 offset:46080\n"
    "buffer_load_dwordx4 v[120:123], v1, s[44:47], s62 offen offset:0 sc1\n"
    "s_waitcnt lgkmcnt(8)\n"
    "v_mfma_f32_32x32x16_bf16 v[64:79], a[248:251], v[236:239], v[64:79]\n"
    "ds_read_b64_tr_b16 a[240:241], v10 offset:49152\n"
    "ds_read_b64_tr_b16 a[242:243], v10 offset:50176\n"
    "v_mfma_f32_32x32x16_bf16 v[80:95], a[252:255], v[236:239], v[80:95]\n"
    "ds_read_b64_tr_b16 a[244:245], v11 offset:49152\n"
    "ds_read_b64_tr_b16 a[246:247], v11 offset:50176\n"
    "buffer_load_dwordx4 v[124:127], v1, s[44:47], s62 offen offset:1024 sc1\n"
    "s_waitcnt lgkmcnt(8)\n"
    "v_mfma_f32_32x32x16_bf16 v[64:79], a[224:227], v[240:243], v[64:79]\n"
    "ds_read_b64_tr_b16 a[248:249], v10 offset:53248\n"
    "ds_read_b64_tr_b16 a[250:251], v10 offset:54272\n"
    "v_mfma_f32_32x32x16_bf16 v[64:79], v[100:103], v[104:107], v[64:79]\n"
    "v_mfma_f32_32x32x16_bf16 v[80:95], a[228:231], v[240:243], v[80:95]\n"
    "ds_read_b64_tr_b16 a[252:253], v11 offset:53248\n"
    "ds_read_b64_tr_b16 a[254:255], v11 offset:54272\n"
    "v_mfma_f32_32x32x16_bf16 v[80:95], v[102:105], v[104:107], v[80:95]\n"
    "buffer_load_dwordx4 v[128:131], v1, s[44:47], s62 offen offset:2048 sc1\n"
    "s_waitcnt lgkmcnt(8)\n"
    "v_mfma_f32_32x32x16_bf16 v[64:79], a[232:235], v[244:247], v[64:79]\n"
    "v_xor_b32 v8, 64, v8\n"
    "v_xor_b32 v9, 64, v9\n"
    "ds_read_b64_tr_b16 a[224:225], v8 offset:0\n"
    "ds_read_b64_tr_b16 a[226:227], v8 offset:1024\n"
    "v_mfma_f32_32x32x16_bf16 v[80:95], a[236:239], v[244:247], v[80:95]\n"
    "ds_read_b64_tr_b16 a[228:229], v9 offset:0\n"
    "ds_read_b64_tr_b16 a[230:231], v9 offset:1024\n"
    "buffer_load_dwordx4 v[132:135], v1, s[44:47], s62 offen offset:3072 sc1\n"
    "s_waitcnt lgkmcnt(8)\n"
    "v_mfma_f32_32x32x16_bf16 v[64:79], a[240:243], v[248:251], v[64:79]\n"
    "ds_read_b64_tr_b16 a[232:233], v8 offset:4096\n"
    "ds_read_b64_tr_b16 a[234:235], v8 offset:5120\n"
    "v_mfma_f32_32x32x16_bf16 v[80:95], a[244:247], v[248:251], v[80:95]\n"
    "ds_read_b64_tr_b16 a[236:237], v9 offset:4096\n"
    "ds_read_b64_tr_b16 a[238:239], v9 offset:5120\n"
    "buffer_load_dwordx4 v[224:227], v140, s[44:47], s89 offen offset:0 sc1\n"
    "s_waitcnt lgkmcnt(8)\n"
    "v_mfma_f32_32x32x16_bf16 v[64:79], a[248:251], v[252:255], v[64:79]\n"
    "ds_read_b64_tr_b16 a[240:241], v8 offset:8192\n"
    "ds_read_b64_tr_b16 a[242:243], v8 offset:9216\n"
    "v_mfma_f32_32x32x16_bf16 v[80:95], a[252:255], v[252:255], v[80:95]\n"
    "ds_read_b64_tr_b16 a[244:245], v9 offset:8192\n"
    "ds_read_b64_tr_b16 a[246:247], v9 offset:9216\n"
    "buffer_load_dwordx4 v[228:231], v140, s[44:47], s89 offen offset:1024 sc1\n"
    "s_waitcnt vmcnt(27) lgkmcnt(8)\n"
    "v_mfma_f32_32x32x16_bf16 v[32:47], a[224:227], v[144:147], 0\n"
    "ds_read_b64_tr_b16 a[248:249], v8 offset:12288\n"
    "ds_read_b64_tr_b16 a[250:251], v8 offset:13312\n"
    "v_mfma_f32_32x32x16_bf16 v[48:63], a[228:231], v[144:147], 0\n"
    "ds_read_b64_tr_b16 a[252:253], v9 offset:12288\n"
    "ds_read_b64_tr_b16 a[254:255], v9 offset:13312\n"
    "buffer_load_dwordx4 v[232:235], v140, s[44:47], s89 offen offset:2048 sc1\n"
    "v_mul_f32 v64, v64, v19\n"
    "s_waitcnt vmcnt(27) lgkmcnt(8)\n"
    "v_mfma_f32_32x32x16_bf16 v[32:47], a[232:235], v[148:151], v[32:47]\n"
    "ds_read_b64_tr_b16 a[224:225], v8 offset:16384\n"
    "ds_read_b64_tr_b16 a[226:227], v8 offset:17408\n"
    "s_waitcnt vmcnt(12)\n"
    "buffer_store_dword v16, v2, s[44:47], s72 offen offset:0 sc1\n"
    "v_mul_f32 v64, v64, v72\n"
    "v_mfma_f32_32x32x16_bf16 v[48:63], a[236:239], v[148:151], v[48:63]\n"
    "ds_read_b64_tr_b16 a[228:229], v9 offset:16384\n"
    "ds_read_b64_tr_b16 a[230:231], v9 offset:17408\n"
    "buffer_load_dwordx4 v[236:239], v140, s[44:47], s89 offen offset:3072 sc1\n"
    "v_mul_f32 v65, v65, v19\n"
    "s_waitcnt lgkmcnt(8)\n"
    "v_mfma_f32_32x32x16_bf16 v[32:47], a[240:243], v[152:155], v[32:47]\n"
    "ds_read_b64_tr_b16 a[232:233], v8 offset:20480\n"
    "ds_read_b64_tr_b16 a[234:235], v8 offset:21504\n"
    "v_mul_f32 v65, v65, v73\n"
    "v_cvt_pk_bf16_f32 v112, v64, v65\n"
    "v_mul_f32 v66, v66, v19\n"
    "v_mfma_f32_32x32x16_bf16 v[48:63], a[244:247], v[152:155], v[48:63]\n"
    "ds_read_b64_tr_b16 a[236:237], v9 offset:20480\n"
    "ds_read_b64_tr_b16 a[238:239], v9 offset:21504\n"
    "buffer_load_dwordx4 v[240:243], v140, s[44:47], s90 offen offset:0 sc1\n"
    "v_mul_f32 v66, v66, v74\n"
    "s_waitcnt lgkmcnt(8)\n"
    "v_mfma_f32_32x32x16_bf16 v[32:47], a[248:251], v[156:159], v[32:47]\n"
    "ds_read_b64_tr_b16 a[240:241], v8 offset:24576\n"
    "ds_read_b64_tr_b16 a[242:243], v8 offset:25600\n"
    "v_mul_f32 v67, v67, v19\n"
    "v_mul_f32 v67, v67, v75\n"
    "v_cvt_pk_bf16_f32 v113, v66, v67\n"
    "v_mfma_f32_32x32x16_bf16 v[48:63], a[252:255], v[156:159], v[48:63]\n"
    "ds_read_b64_tr_b16 a[244:245], v9 offset:24576\n"
    "ds_read_b64_tr_b16 a[246:247], v9 offset:25600\n"
    "buffer_load_dwordx4 v[244:247], v140, s[44:47], s90 offen offset:1024 sc1\n"
    "v_mul_f32 v68, v68, v19\n"
    "s_waitcnt lgkmcnt(8)\n"
    "v_mfma_f32_32x32x16_bf16 v[32:47], a[224:227], v[160:163], v[32:47]\n"
    "ds_read_b64_tr_b16 a[248:249], v8 offset:28672\n"
    "ds_read_b64_tr_b16 a[250:251], v8 offset:29696\n"
    "buffer_load_dword v23, v14, s[44:47], s4 offen sc1\n"
    "v_mul_f32 v68, v68, v76\n"
    "v_mul_f32 v69, v69, v19\n"
    "v_mfma_f32_32x32x16_bf16 v[48:63], a[228:231], v[160:163], v[48:63]\n"
    "ds_read_b64_tr_b16 a[252:253], v9 offset:28672\n"
    "ds_read_b64_tr_b16 a[254:255], v9 offset:29696\n"
    "v_mul_f32 v69, v69, v77\n"
    "v_cvt_pk_bf16_f32 v114, v68, v69\n"
    "s_waitcnt lgkmcnt(8)\n"
    "v_mfma_f32_32x32x16_bf16 v[32:47], a[232:235], v[164:167], v[32:47]\n"
    "ds_read_b64_tr_b16 a[224:225], v8 offset:32768\n"
    "ds_read_b64_tr_b16 a[226:227], v8 offset:33792\n"
    "v_mul_f32 v70, v70, v19\n"
    "v_mul_f32 v70, v70, v78\n"
    "v_mul_f32 v71, v71, v19\n"
    "v_mfma_f32_32x32x16_bf16 v[48:63], a[236:239], v[164:167], v[48:63]\n"
    "ds_read_b64_tr_b16 a[228:229], v9 offset:32768\n"
    "ds_read_b64_tr_b16 a[230:231], v9 offset:33792\n"
    "buffer_load_dwordx4 v[248:251], v140, s[44:47], s90 offen offset:2048 sc1\n"
    "v_mul_f32 v71, v71, v79\n"
    "s_waitcnt lgkmcnt(8)\n"
    "v_mfma_f32_32x32x16_bf16 v[32:47], a[240:243], v[168:171], v[32:47]\n"
    "ds_read_b64_tr_b16 a[232:233], v8 offset:36864\n"
    "ds_read_b64_tr_b16 a[234:235], v8 offset:37888\n"
    "v_cvt_pk_bf16_f32 v115, v70, v71\n"
    "v_mul_f32 v80, v80, v19\n"
    "v_mfma_f32_32x32x16_bf16 v[48:63], a[244:247], v[168:171], v[48:63]\n"
    "ds_read_b64_tr_b16 a[236:237], v9 offset:36864\n"
    "ds_read_b64_tr_b16 a[238:239], v9 offset:37888\n"
    "buffer_load_dwordx4 v[252:255], v140, s[44:47], s90 offen offset:3072 sc1\n"
    "v_mul_f32 v80, v80, v88\n"
    "s_waitcnt lgkmcnt(8)\n"
    "v_mfma_f32_32x32x16_bf16 v[32:47], a[248:251], v[172:175], v[32:47]\n"
    "ds_read_b64_tr_b16 a[240:241], v8 offset:40960\n"
    "ds_read_b64_tr_b16 a[242:243], v8 offset:41984\n"
    "buffer_store_dwordx4 v[112:115], v1, s[44:47], s63 offen offset:2048 sc1\n"
    "v_mul_f32 v81, v81, v19\n"
    "v_mul_f32 v81, v81, v89\n"
    "ds_read_b64_tr_b16 a[112:113], v136 offset:6144\n"
    "v_mfma_f32_32x32x16_bf16 v[48:63], a[252:255], v[172:175], v[48:63]\n"
    "ds_read_b64_tr_b16 a[244:245], v9 offset:40960\n"
    "ds_read_b64_tr_b16 a[246:247], v9 offset:41984\n"
    "v_cvt_pk_bf16_f32 v116, v80, v81\n"
    "v_mul_f32 v82, v82, v19\n"
    "ds_read_b64_tr_b16 a[114:115], v136 offset:7168\n"
    "s_waitcnt lgkmcnt(10)\n"
    "v_mfma_f32_32x32x16_bf16 v[32:47], a[224:227], v[176:179], v[32:47]\n"
    "ds_read_b64_tr_b16 a[248:249], v8 offset:45056\n"
    "ds_read_b64_tr_b16 a[250:251], v8 offset:46080\n"
    "v_mul_f32 v82, v82, v90\n"
    "v_mul_f32 v83, v83, v19\n"
    "v_mul_f32 v83, v83, v91\n"
    "s_mul_i32 s5, s86, 14\n"
    "s_add_u32 s5, s5, 28672\n"
    "s_add_u32 m0, s75, 4096\n"
    "s_nop 0\n"
    "buffer_load_dwordx4 v4, s[36:39], s5 offen lds\n"
    "v_mfma_f32_32x32x16_bf16 v[48:63], a[228:231], v[176:179], v[48:63]\n"
    "ds_read_b64_tr_b16 a[252:253], v9 offset:45056\n"
    "ds_read_b64_tr_b16 a[254:255], v9 offset:46080\n"
    "v_cvt_pk_bf16_f32 v117, v82, v83\n"
    "v_mul_f32 v84, v84, v19\n"
    "ds_read_b64_tr_b16 a[116:117], v137 offset:6144\n"
    "s_waitcnt lgkmcnt(11)\n"
    "v_mfma_f32_32x32x16_bf16 v[32:47], a[232:235], v[180:183], v[32:47]\n"
    "ds_read_b64_tr_b16 a[224:225], v8 offset:49152\n"
    "ds_read_b64_tr_b16 a[226:227], v8 offset:50176\n"
    "v_mul_f32 v84, v84, v92\n"
    "v_mul_f32 v85, v85, v19\n"
    "v_mul_f32 v85, v85, v93\n"
    "buffer_load_dwordx4 v143, s[36:39], s5 offen offset:1024 lds\n"
    "v_mfma_f32_32x32x16_bf16 v[48:63], a[236:239], v[180:183], v[48:63]\n"
    "ds_read_b64_tr_b16 a[228:229], v9 offset:49152\n"
    "ds_read_b64_tr_b16 a[230:231], v9 offset:50176\n"
    "v_cvt_pk_bf16_f32 v118, v84, v85\n"
    "v_mul_f32 v86, v86, v19\n"
    "ds_read_b64_tr_b16 a[118:119], v137 offset:7168\n"
    "s_waitcnt lgkmcnt(11)\n"
    "v_mfma_f32_32x32x16_bf16 v[32:47], a[240:243], v[184:187], v[32:47]\n"
    "ds_read_b64_tr_b16 a[232:233], v8 offset:53248\n"
    "ds_read_b64_tr_b16 a[234:235], v8 offset:54272\n"
    "v_mul_f32 v86, v86, v94\n"
    "v_mul_f32 v87, v87, v19\n"
    "v_mul_f32 v87, v87, v95\n"
    "v_mfma_f32_32x32x16_bf16 v[48:63], a[244:247], v[184:187], v[48:63]\n"
    "ds_read_b64_tr_b16 a[236:237], v9 offset:53248\n"
    "ds_read_b64_tr_b16 a[238:239], v9 offset:54272\n"
    "v_cvt_pk_bf16_f32 v119, v86, v87\n"
    "buffer_store_dwordx4 v[116:119], v1, s[44:47], s63 offen offset:3072 sc1\n"
    "ds_read_b64_tr_b16 a[120:121], v136 offset:8192\n"
    "s_waitcnt lgkmcnt(11)\n"
    "v_mfma_f32_32x32x16_bf16 v[32:47], a[248:251], v[188:191], v[32:47]\n"
    "v_xor_b32 v10, 64, v10\n"
    "v_xor_b32 v11, 64, v11\n"
    "ds_read_b64_tr_b16 a[240:241], v10 offset:0\n"
    "ds_read_b64_tr_b16 a[242:243], v10 offset:1024\n"
    "s_waitcnt vmcnt(17)\n"
    "v_add_f32 v20, v120, v121\n"
    "s_waitcnt lgkmcnt(7)\n"
    "s_add_u32 s5, s5, 14336\n"
    "s_add_u32 m0, s75, 6144\n"
    "s_nop 0\n"
    "buffer_load_dwordx4 v4, s[36:39], s5 offen lds\n"
    "v_mfma_f32_32x32x16_bf16 v[48:63], a[252:255], v[188:191], v[48:63]\n"
    "ds_read_b64_tr_b16 a[244:245], v11 offset:0\n"
    "ds_read_b64_tr_b16 a[246:247], v11 offset:1024\n"
    "v_add_f32 v21, v122, v123\n"
    "s_waitcnt vmcnt(17)\n"
    "v_add_f32 v20, v20, v124\n"
    "ds_read_b64_tr_b16 a[122:123], v136 offset:9216\n"
    "v_mfma_f32_32x32x16_bf16 v[32:47], a[224:227], v[192:195], v[32:47]\n"
    "ds_read_b64_tr_b16 a[248:249], v10 offset:4096\n"
    "ds_read_b64_tr_b16 a[250:251], v10 offset:5120\n"
    "v_add_f32 v21, v21, v125\n"
    "v_add_f32 v20, v20, v126\n"
    "v_add_f32 v21, v21, v127\n"
    "buffer_load_dwordx4 v143, s[36:39], s5 offen offset:1024 lds\n"
    "v_mfma_f32_32x32x16_bf16 v[48:63], a[228:231], v[192:195], v[48:63]\n"
    "ds_read_b64_tr_b16 a[252:253], v11 offset:4096\n"
    "ds_read_b64_tr_b16 a[254:255], v11 offset:5120\n"
    "s_waitcnt vmcnt(17)\n"
    "v_add_f32 v20, v20, v128\n"
    "v_add_f32 v21, v21, v129\n"
    "ds_read_b64_tr_b16 a[124:125], v137 offset:8192\n"
    "s_waitcnt lgkmcnt(11)\n"
    "v_mfma_f32_32x32x16_bf16 v[32:47], a[232:235], v[196:199], v[32:47]\n"
    "ds_read_b64_tr_b16 a[224:225], v10 offset:8192\n"
    "ds_read_b64_tr_b16 a[226:227], v10 offset:9216\n"
    "v_add_f32 v20, v20, v130\n"
    "v_add_f32 v21, v21, v131\n"
    "s_waitcnt vmcnt(16)\n"
    "v_add_f32 v20, v20, v132\n"
    "ds_read_b64_tr_b16 a[126:127], v137 offset:9216\n"
    "v_mfma_f32_32x32x16_bf16 v[48:63], a[236:239], v[196:199], v[48:63]\n"
    "ds_read_b64_tr_b16 a[228:229], v11 offset:8192\n"
    "ds_read_b64_tr_b16 a[230:231], v11 offset:9216\n"
    "v_add_f32 v21, v21, v133\n"
    "v_add_f32 v20, v20, v21\n"
    "s_waitcnt lgkmcnt(11)\n"
    "v_mfma_f32_32x32x16_bf16 v[32:47], a[240:243], v[200:203], v[32:47]\n"
    "ds_read_b64_tr_b16 a[232:233], v10 offset:12288\n"
    "ds_read_b64_tr_b16 a[234:235], v10 offset:13312\n"
    "s_waitcnt vmcnt(8)\n"
    "v_cmp_ne_u32 vcc, s56, v23\n"
    "s_cbranch_vccnz L_retry_21_%=\n"
    "L_go_22_%=:\n"
    "v_mov_b32 v21, v20\n"
    "s_nop 1\n"
    "v_permlane32_swap_b32 v20, v21\n"
    "ds_read_b64_tr_b16 a[128:129], v136 offset:0\n"
    "v_mfma_f32_32x32x16_bf16 v[48:63], a[244:247], v[200:203], v[48:63]\n"
    "ds_read_b64_tr_b16 a[236:237], v11 offset:12288\n"
    "ds_read_b64_tr_b16 a[238:239], v11 offset:13312\n"
    "v_mul_f32 v20, -0.002232142857142857, v20\n"
    "v_mul_f32 v21, 0.002232142857142857, v21\n"
    "s_waitcnt lgkmcnt(7)\n"
    "s_mul_i32 s5, s87, 14\n"
    "s_add_u32 s5, s5, 28672\n"
    "s_add_u32 m0, s75, 8192\n"
    "s_nop 0\n"
    "buffer_load_dwordx4 v4, s[36:39], s5 offen lds\n"
    "v_mfma_f32_32x32x16_bf16 v[32:47], a[248:251], v[204:207], v[32:47]\n"
    "ds_read_b64_tr_b16 a[240:241], v10 offset:16384\n"
    "ds_read_b64_tr_b16 a[242:243], v10 offset:17408\n"
    "v_fma_f32 v21, -v20, v20, v21\n"
    "v_max_f32 v21, 0, v21\n"
    "v_add_f32 v21, 1e-05, v21\n"
    "buffer_load_dwordx4 v143, s[36:39], s5 offen offset:1024 lds\n"
    "v_mfma_f32_32x32x16_bf16 v[48:63], a[252:255], v[204:207], v[48:63]\n"
    "ds_read_b64_tr_b16 a[244:245], v11 offset:16384\n"
    "ds_read_b64_tr_b16 a[246:247], v11 offset:17408\n"
    "v_rcp_f32 v21, v21\n"
    "ds_read_b64_tr_b16 a[130:131], v136 offset:1024\n"
    "s_waitcnt lgkmcnt(10)\n"
    "v_mfma_f32_32x32x16_bf16 v[32:47], a[224:227], v[208:211], v[32:47]\n"
    "ds_read_b64_tr_b16 a[248:249], v10 offset:20480\n"
    "ds_read_b64_tr_b16 a[250:251], v10 offset:21504\n"
    "v_cndmask_b32 v20, 0, v20, s[78:79]\n"
    "v_cvt_pk_bf16_f32 v108, v20, v20\n"
    "v_lshlrev_b32 v109, 16, v108\n"
    "ds_read_b64_tr_b16 a[132:133], v137 offset:0\n"
    "v_mfma_f32_32x32x16_bf16 v[48:63], a[228:231], v[208:211], v[48:63]\n"
    "ds_read_b64_tr_b16 a[252:253], v11 offset:20480\n"
    "ds_read_b64_tr_b16 a[254:255], v11 offset:21504\n"
    "v_sub_f32 v109, v20, v109\n"
    "v_cvt_pk_bf16_f32 v109, v109, 0\n"
    "ds_read_b64_tr_b16 a[134:135], v137 offset:1024\n"
    "s_waitcnt lgkmcnt(11)\n"
    "v_mfma_f32_32x32x16_bf16 v[32:47], a[232:235], v[212:215], v[32:47]\n"
    "ds_read_b64_tr_b16 a[224:225], v10 offset:24576\n"
    "ds_read_b64_tr_b16 a[226:227], v10 offset:25600\n"
    "v_mov_b32 v110, 0\n"
    "v_mov_b32 v111, 0\n"
    "v_mfma_f32_32x32x16_bf16 v[48:63], a[236:239], v[212:215], v[48:63]\n"
    "ds_read_b64_tr_b16 a[228:229], v11 offset:24576\n"
    "ds_read_b64_tr_b16 a[230:231], v11 offset:25600\n"
    "ds_read_b64_tr_b16 a[136:137], v136 offset:2048\n"
    "ds_read_b64_tr_b16 a[138:139], v136 offset:3072\n"
    "s_waitcnt lgkmcnt(13)\n"
    "v_mfma_f32_32x32x16_bf16 v[32:47], a[240:243], v[216:219], v[32:47]\n"
    "ds_read_b64_tr_b16 a[232:233], v10 offset:28672\n"
    "ds_read_b64_tr_b16 a[234:235], v10 offset:29696\n"
    "s_waitcnt lgkmcnt(8)\n"
    "s_add_u32 s5, s5, 14336\n"
    "s_add_u32 m0, s75, 0\n"
    "s_nop 0\n"
    "buffer_load_dwordx4 v4, s[36:39], s5 offen lds\n"
    "ds_read_b64_tr_b16 a[140:141], v137 offset:2048\n"
    "ds_read_b64_tr_b16 a[142:143], v137 offset:3072\n"
    "v_mfma_f32_32x32x16_bf16 v[48:63], a[244:247], v[216:219], v[48:63]\n"
    "ds_read_b64_tr_b16 a[236:237], v11 offset:28672\n"
    "ds_read_b64_tr_b16 a[238:239], v11 offset:29696\n"
    "v_mfma_f32_32x32x16_bf16 v[32:47], a[248:251], v[220:223], v[32:47]\n"
    "ds_read_b64_tr_b16 a[240:241], v10 offset:32768\n"
    "ds_read_b64_tr_b16 a[242:243], v10 offset:33792\n"
    "buffer_load_dword v28, v13, s[44:47], s65 offen offset:0 sc1\n"
    "v_mfma_f32_32x32x16_bf16 v[48:63], a[252:255], v[220:223], v[48:63]\n"
    "ds_read_b64_tr_b16 a[244:245], v11 offset:32768\n"
    "ds_read_b64_tr_b16 a[246:247], v11 offset:33792\n"
    "s_waitcnt vmcnt(7)\n"
    "ds_read_b64_tr_b16 a[144:145], v136 offset:4096\n"
    "ds_read_b64_tr_b16 a[146:147], v136 offset:5120\n"
    "s_waitcnt lgkmcnt(14)\n"
    "v_mfma_f32_32x32x16_bf16 v[32:47], a[224:227], v[224:227], v[32:47]\n"
    "ds_read_b64_tr_b16 a[248:249], v10 offset:36864\n"
    "ds_read_b64_tr_b16 a[250:251], v10 offset:37888\n"
    "buffer_load_dwordx4 v143, s[36:39], s5 offen offset:1024 lds\n"
    "ds_read_b64_tr_b16 a[148:149], v137 offset:4096\n"
    "ds_read_b64_tr_b16 a[150:151], v137 offset:5120\n"
    "v_mfma_f32_32x32x16_bf16 v[48:63], a[228:231], v[224:227], v[48:63]\n"
    "ds_read_b64_tr_b16 a[252:253], v11 offset:36864\n"
    "ds_read_b64_tr_b16 a[254:255], v11 offset:37888\n"
    "s_waitcnt lgkmcnt(12)\n"
    "v_mfma_f32_32x32x16_bf16 v[32:47], a[232:235], v[228:231], v[32:47]\n"
    "ds_read_b64_tr_b16 a[224:225], v10 offset:40960\n"
    "ds_read_b64_tr_b16 a[226:227], v10 offset:41984\n"
    "s_mul_i32 s5, s88, 14\n"
    "s_add_u32 s5, s5, 28672\n"
    "s_add_u32 m0, s75, 2048\n"
    "s_nop 0\n"
    "buffer_load_dwordx4 v4, s[36:39], s5 offen lds\n"
    "v_mfma_f32_32x32x16_bf16 v[48:63], a[236:239], v[228:231], v[48:63]\n"
    "ds_read_b64_tr_b16 a[228:229], v11 offset:40960\n"
    "ds_read_b64_tr_b16 a[230:231], v11 offset:41984\n"
    "s_waitcnt lgkmcnt(12)\n"
    "v_mfma_f32_32x32x16_bf16 v[32:47], a[240:243], v[232:235], v[32:47]\n"
    "ds_read_b64_tr_b16 a[232:233], v10 offset:45056\n"
    "ds_read_b64_tr_b16 a[234:235], v10 offset:46080\n"
    "buffer_load_dwordx4 v143, s[36:39], s5 offen offset:1024 lds\n"
    "v_mfma_f32_32x32x16_bf16 v[48:63], a[244:247], v[232:235], v[48:63]\n"
    "ds_read_b64_tr_b16 a[236:237], v11 offset:45056\n"
    "ds_read_b64_tr_b16 a[238:239], v11 offset:46080\n"
    "s_waitcnt lgkmcnt(8)\n"
    "v_mfma_f32_32x32x16_bf16 v[32:47], a[248:251], v[236:239], v[32:47]\n"
    "ds_read_b64_tr_b16 a[240:241], v10 offset:49152\n"
    "ds_read_b64_tr_b16 a[242:243], v10 offset:50176\n"
    "s_add_u32 s5, s5, 14336\n"
    "s_add_u32 m0, s75, 4096\n"
    "s_nop 0\n"
    "buffer_load_dwordx4 v4, s[36:39], s5 offen lds\n"
    "v_mfma_f32_32x32x16_bf16 v[48:63], a[252:255], v[236:239], v[48:63]\n"
    "ds_read_b64_tr_b16 a[244:245], v11 offset:49152\n"
    "ds_read_b64_tr_b16 a[246:247], v11 offset:50176\n"
    "s_waitcnt vmcnt(10)\n"
    "buffer_store_dword v16, v2, s[44:47], s72 offen offset:4 sc1\n"
    "s_waitcnt vmcnt(9) lgkmcnt(8)\n"
    "v_mfma_f32_32x32x16_bf16 v[32:47], a[224:227], v[240:243], v[32:47]\n"
    "ds_read_b64_tr_b16 a[248:249], v10 offset:53248\n"
    "ds_read_b64_tr_b16 a[250:251], v10 offset:54272\n"
    "v_mfma_f32_32x32x16_bf16 v[32:47], v[96:99], v[108:111], v[32:47]\n"
    "buffer_load_dwordx4 v[120:123], v1, s[44:47], s67 offen sc1\n"
    "ds_read_b64_tr_b16 a[152:153], v136 offset:6144\n"
    "ds_read_b64_tr_b16 a[154:155], v136 offset:7168\n"
    "v_mfma_f32_32x32x16_bf16 v[48:63], a[228:231], v[240:243], v[48:63]\n"
    "ds_read_b64_tr_b16 a[252:253], v11 offset:53248\n"
    "ds_read_b64_tr_b16 a[254:255], v11 offset:54272\n"
    "v_mfma_f32_32x32x16_bf16 v[48:63], v[98:101], v[108:111], v[48:63]\n"
    "ds_read_b64_tr_b16 a[156:157], v137 offset:6144\n"
    "ds_read_b64_tr_b16 a[158:159], v137 offset:7168\n"
    "s_waitcnt lgkmcnt(12)\n"
    "v_mfma_f32_32x32x16_bf16 v[32:47], a[232:235], v[244:247], v[32:47]\n"
    "v_xor_b32 v8, 64, v8\n"
    "v_xor_b32 v9, 64, v9\n"
    "ds_read_b64_tr_b16 a[224:225], v8 offset:0\n"
    "ds_read_b64_tr_b16 a[226:227], v8 offset:1024\n"
    "buffer_load_dwordx4 v[124:127], v1, s[44:47], s68 offen sc1\n"
    "v_mfma_f32_32x32x16_bf16 v[48:63], a[236:239], v[244:247], v[48:63]\n"
    "ds_read_b64_tr_b16 a[228:229], v9 offset:0\n"
    "ds_read_b64_tr_b16 a[230:231], v9 offset:1024\n"
    "s_waitcnt lgkmcnt(12)\n"
    "v_mfma_f32_32x32x16_bf16 v[32:47], a[240:243], v[248:251], v[32:47]\n"
    "ds_read_b64_tr_b16 a[232:233], v8 offset:4096\n"
    "ds_read_b64_tr_b16 a[234:235], v8 offset:5120\n"
    "buffer_load_dwordx4 v143, s[36:39], s5 offen offset:1024 lds\n"
    "v_mfma_f32_32x32x16_bf16 v[48:63], a[244:247], v[248:251], v[48:63]\n"
    "ds_read_b64_tr_b16 a[236:237], v9 offset:4096\n"
    "ds_read_b64_tr_b16 a[238:239], v9 offset:5120\n"
    "s_waitcnt lgkmcnt(10)\n"
    "v_mfma_f32_32x32x16_bf16 v[32:47], a[248:251], v[252:255], v[32:47]\n"
    "ds_read_b64_tr_b16 a[240:241], v8 offset:8192\n"
    "ds_read_b64_tr_b16 a[242:243], v8 offset:9216\n"
    "s_waitcnt vmcnt(8)\n"
    "v_cmp_ne_u32 vcc, s56, v28\n"
    "s_cbranch_vccnz L_retry_25_%=\n"
    "L_go_26_%=:\n"
    "s_waitcnt lgkmcnt(10)\n"
    "s_mul_i32 s5, s89, 14\n"
    "s_add_u32 s5, s5, 28672\n"
    "s_add_u32 m0, s75, 6144\n"
    "s_nop 0\n"
    "buffer_load_dwordx4 v4, s[36:39], s5 offen lds\n"
    "v_mfma_f32_32x32x16_bf16 v[48:63], a[252:255], v[252:255], v[48:63]\n"
    "ds_read_b64_tr_b16 a[244:245], v9 offset:8192\n"
    "ds_read_b64_tr_b16 a[246:247], v9 offset:9216\n"
    "s_waitcnt lgkmcnt(8)\n"
    "v_mfma_f32_32x32x16_bf16 v[64:79], a[224:227], v[144:147], 0\n"
    "ds_read_b64_tr_b16 a[248:249], v8 offset:12288\n"
    "ds_read_b64_tr_b16 a[250:251], v8 offset:13312\n"
    "v_mfma_f32_32x32x16_bf16 v[80:95], a[228:231], v[144:147], 0\n"
    "ds_read_b64_tr_b16 a[252:253], v9 offset:12288\n"
    "ds_read_b64_tr_b16 a[254:255], v9 offset:13312\n"
    "v_mul_f32 v32, v32, v21\n"
    "v_mul_f32 v32, v32, v40\n"
    "buffer_load_dwordx4 v[144:147], v141, s[44:47], s84 offen offset:0 sc1\n"
    "s_waitcnt lgkmcnt(8)\n"
    "v_mfma_f32_32x32x16_bf16 v[64:79], a[232:235], v[148:151], v[64:79]\n"
    "ds_read_b64_tr_b16 a[224:225], v8 offset:16384\n"
    "ds_read_b64_tr_b16 a[226:227], v8 offset:17408\n"
    "v_mul_f32 v33, v33, v21\n"
    "v_mul_f32 v33, v33, v41\n"
    "v_cvt_pk_bf16_f32 v112, v32, v33\n"
    "v_mfma_f32_32x32x16_bf16 v[80:95], a[236:239], v[148:151], v[80:95]\n"
    "ds_read_b64_tr_b16 a[228:229], v9 offset:16384\n"
    "ds_read_b64_tr_b16 a[230:231], v9 offset:17408\n"
    "v_mul_f32 v34, v34, v21\n"
    "v_mul_f32 v34, v34, v42\n"
    "buffer_load_dwordx4 v[148:151], v141, s[44:47], s84 offen offset:1024 sc1\n"
    "s_waitcnt lgkmcnt(8)\n"
    "v_mfma_f32_32x32x16_bf16 v[64:79], a[240:243], v[152:155], v[64:79]\n"
    "ds_read_b64_tr_b16 a[232:233], v8 offset:20480\n"
    "ds_read_b64_tr_b16 a[234:235], v8 offset:21504\n"
    "v_mul_f32 v35, v35, v21\n"
    "v_mul_f32 v35, v35, v43\n"
    "v_cvt_pk_bf16_f32 v113, v34, v35\n"
    "v_mfma_f32_32x32x16_bf16 v[80:95], a[244:247], v[152:155], v[80:95]\n"
    "ds_read_b64_tr_b16 a[236:237], v9 offset:20480\n"
    "ds_read_b64_tr_b16 a[238:239], v9 offset:21504\n"
    "v_mul_f32 v36, v36, v21\n"
    "v_mul_f32 v36, v36, v44\n"
    "buffer_load_dwordx4 v[152:155], v141, s[44:47], s85 offen offset:0 sc1\n"
    "s_waitcnt lgkmcnt(8)\n"
    "v_mfma_f32_32x32x16_bf16 v[64:79], a[248:251], v[156:159], v[64:79]\n"
    "ds_read_b64_tr_b16 a[240:241], v8 offset:24576\n"
    "ds_read_b64_tr_b16 a[242:243], v8 offset:25600\n"
    "v_mul_f32 v37, v37, v21\n"
    "v_mul_f32 v37, v37, v45\n"
    "v_cvt_pk_bf16_f32 v114, v36, v37\n"
    "v_mfma_f32_32x32x16_bf16 v[80:95], a[252:255], v[156:159], v[80:95]\n"
    "ds_read_b64_tr_b16 a[244:245], v9 offset:24576\n"
    "ds_read_b64_tr_b16 a[246:247], v9 offset:25600\n"
    "v_mul_f32 v38, v38, v21\n"
    "v_mul_f32 v38, v38, v46\n"
    "buffer_load_dwordx4 v[156:159], v141, s[44:47], s85 offen offset:1024 sc1\n"
    "s_waitcnt lgkmcnt(8)\n"
    "v_mfma_f32_32x32x16_bf16 v[64:79], a[224:227], v[160:163], v[64:79]\n"
    "ds_read_b64_tr_b16 a[248:249], v8 offset:28672\n"
    "ds_read_b64_tr_b16 a[250:251], v8 offset:29696\n"
    "v_mul_f32 v39, v39, v21\n"
    "v_mul_f32 v39, v39, v47\n"
    "v_cvt_pk_bf16_f32 v115, v38, v39\n"
    "v_mfma_f32_32x32x16_bf16 v[80:95], a[228:231], v[160:163], v[80:95]\n"
    "ds_read_b64_tr_b16 a[252:253], v9 offset:28672\n"
    "ds_read_b64_tr_b16 a[254:255], v9 offset:29696\n"
    "buffer_store_dwordx4 v[112:115], v1, s[44:47], s64 offen offset:0 sc1\n"
    "s_waitcnt lgkmcnt(8)\n"
    "v_mfma_f32_32x32x16_bf16 v[64:79], a[232:235], v[164:167], v[64:79]\n"
    "ds_read_b64_tr_b16 a[224:225], v8 offset:32768\n"
    "ds_read_b64_tr_b16 a[226:227], v8 offset:33792\n"
    "v_mul_f32 v48, v48, v21\n"
    "v_mul_f32 v48, v48, v56\n"
    "v_mul_f32 v49, v49, v21\n"
    "v_mfma_f32_32x32x16_bf16 v[80:95], a[236:239], v[164:167], v[80:95]\n"
    "ds_read_b64_tr_b16 a[228:229], v9 offset:32768\n"
    "ds_read_b64_tr_b16 a[230:231], v9 offset:33792\n"
    "v_mul_f32 v49, v49, v57\n"
    "v_cvt_pk_bf16_f32 v116, v48, v49\n"
    "buffer_load_dwordx4 v[160:163], v141, s[44:47], s86 offen offset:0 sc1\n"
    "s_waitcnt lgkmcnt(8)\n"
    "v_mfma_f32_32x32x16_bf16 v[64:79], a[240:243], v[168:171], v[64:79]\n"
    "ds_read_b64_tr_b16 a[232:233], v8 offset:36864\n"
    "ds_read_b64_tr_b16 a[234:235], v8 offset:37888\n"
    "v_mul_f32 v50, v50, v21\n"
    "v_mul_f32 v50, v50, v58\n"
    "v_mul_f32 v51, v51, v21\n"
    "v_mfma_f32_32x32x16_bf16 v[80:95], a[244:247], v[168:171], v[80:95]\n"
    "ds_read_b64_tr_b16 a[236:237], v9 offset:36864\n"
    "ds_read_b64_tr_b16 a[238:239], v9 offset:37888\n"
    "v_mul_f32 v51, v51, v59\n"
    "v_cvt_pk_bf16_f32 v117, v50, v51\n"
    "buffer_load_dwordx4 v[164:167], v141, s[44:47], s86 offen offset:1024 sc1\n"
    "s_waitcnt lgkmcnt(8)\n"
    "v_mfma_f32_32x32x16_bf16 v[64:79], a[248:251], v[172:175], v[64:79]\n"
    "ds_read_b64_tr_b16 a[240:241], v8 offset:40960\n"
    "ds_read_b64_tr_b16 a[242:243], v8 offset:41984\n"
    "v_mul_f32 v52, v52, v21\n"
    "v_mul_f32 v52, v52, v60\n"
    "v_mul_f32 v53, v53, v21\n"
    "v_mfma_f32_32x32x16_bf16 v[80:95], a[252:255], v[172:175], v[80:95]\n"
    "ds_read_b64_tr_b16 a[244:245], v9 offset:40960\n"
    "ds_read_b64_tr_b16 a[246:247], v9 offset:41984\n"
    "v_mul_f32 v53, v53, v61\n"
    "v_cvt_pk_bf16_f32 v118, v52, v53\n"
    "buffer_load_dwordx4 v[168:171], v141, s[44:47], s87 offen offset:0 sc1\n"
    "s_waitcnt lgkmcnt(8)\n"
    "v_mfma_f32_32x32x16_bf16 v[64:79], a[224:227], v[176:179], v[64:79]\n"
    "ds_read_b64_tr_b16 a[248:249], v8 offset:45056\n"
    "ds_read_b64_tr_b16 a[250:251], v8 offset:46080\n"
    "v_mul_f32 v54, v54, v21\n"
    "v_mul_f32 v54, v54, v62\n"
    "v_mul_f32 v55, v55, v21\n"
    "v_mfma_f32_32x32x16_bf16 v[80:95], a[228:231], v[176:179], v[80:95]\n"
    "ds_read_b64_tr_b16 a[252:253], v9 offset:45056\n"
    "ds_read_b64_tr_b16 a[254:255], v9 offset:46080\n"
    "v_mul_f32 v55, v55, v63\n"
    "v_cvt_pk_bf16_f32 v119, v54, v55\n"
    "buffer_store_dwordx4 v[116:119], v1, s[44:47], s64 offen offset:1024 sc1\n"
    "s_waitcnt lgkmcnt(8)\n"
    "v_mfma_f32_32x32x16_bf16 v[64:79], a[232:235], v[180:183], v[64:79]\n"
    "ds_read_b64_tr_b16 a[224:225], v8 offset:49152\n"
    "ds_read_b64_tr_b16 a[226:227], v8 offset:50176\n"
    "v_mfma_f32_32x32x16_bf16 v[80:95], a[236:239], v[180:183], v[80:95]\n"
    "ds_read_b64_tr_b16 a[228:229], v9 offset:49152\n"
    "ds_read_b64_tr_b16 a[230:231], v9 offset:50176\n"
    "buffer_load_dwordx4 v[172:175], v141, s[44:47], s87 offen offset:1024 sc1\n"
    "s_waitcnt lgkmcnt(8)\n"
    "v_mfma_f32_32x32x16_bf16 v[64:79], a[240:243], v[184:187], v[64:79]\n"
    "ds_read_b64_tr_b16 a[232:233], v8 offset:53248\n"
    "ds_read_b64_tr_b16 a[234:235], v8 offset:54272\n"
    "v_mfma_f32_32x32x16_bf16 v[80:95], a[244:247], v[184:187], v[80:95]\n"
    "ds_read_b64_tr_b16 a[236:237], v9 offset:53248\n"
    "ds_read_b64_tr_b16 a[238:239], v9 offset:54272\n"
    "buffer_load_dwordx4 v[176:179], v141, s[44:47], s88 offen offset:0 sc1\n"
    "s_waitcnt lgkmcnt(8)\n"
    "v_mfma_f32_32x32x16_bf16 v[64:79], a[248:251], v[188:191], v[64:79]\n"
    "v_xor_b32 v10, 64, v10\n"
    "v_xor_b32 v11, 64, v11\n"
    "ds_read_b64_tr_b16 a[240:241], v10 offset:0\n"
    "ds_read_b64_tr_b16 a[242:243], v10 offset:1024\n"
    "v_mfma_f32_32x32x16_bf16 v[80:95], a[252:255], v[188:191], v[80:95]\n"
    "ds_read_b64_tr_b16 a[244:245], v11 offset:0\n"
    "ds_read_b64_tr_b16 a[246:247], v11 offset:1024\n"
    "buffer_load_dwordx4 v[180:183], v141, s[44:47], s88 offen offset:1024 sc1\n"
    "s_waitcnt lgkmcnt(8)\n"
    "v_mfma_f32_32x32x16_bf16 v[64:79], a[224:227], v[192:195], v[64:79]\n"
    "ds_read_b64_tr_b16 a[248:249], v10 offset:4096\n"
    "ds_read_b64_tr_b16 a[250:251], v10 offset:5120\n"
    "v_mfma_f32_32x32x16_bf16 v[80:95], a[228:231], v[192:195], v[80:95]\n"
    "ds_read_b64_tr_b16 a[252:253], v11 offset:4096\n"
    "ds_read_b64_tr_b16 a[254:255], v11 offset:5120\n"
    "buffer_load_dwordx4 v[184:187], v141, s[44:47], s89 offen offset:0 sc1\n"
    "s_waitcnt lgkmcnt(8)\n"
    "v_mfma_f32_32x32x16_bf16 v[64:79], a[232:235], v[196:199], v[64:79]\n"
    "ds_read_b64_tr_b16 a[224:225], v10 offset:8192\n"
    "ds_read_b64_tr_b16 a[226:227], v10 offset:9216\n"
    "buffer_load_dword v29, v13, s[44:47], s65 offen offset:4 sc1\n"
    "v_mfma_f32_32x32x16_bf16 v[80:95], a[236:239], v[196:199], v[80:95]\n"
    "ds_read_b64_tr_b16 a[228:229], v11 offset:8192\n"
    "ds_read_b64_tr_b16 a[230:231], v11 offset:9216\n"
    "ds_read_b64_tr_b16 a[160:161], v136 offset:8192\n"
    "ds_read_b64_tr_b16 a[162:163], v136 offset:9216\n"
    "s_waitcnt lgkmcnt(10)\n"
    "v_mfma_f32_32x32x16_bf16 v[64:79], a[240:243], v[200:203], v[64:79]\n"
    "ds_read_b64_tr_b16 a[232:233], v10 offset:12288\n"
    "ds_read_b64_tr_b16 a[234:235], v10 offset:13312\n"
    "ds_read_b64_tr_b16 a[164:165], v137 offset:8192\n"
    "ds_read_b64_tr_b16 a[166:167], v137 offset:9216\n"
    "v_mfma_f32_32x32x16_bf16 v[80:95], a[244:247], v[200:203], v[80:95]\n"
    "ds_read_b64_tr_b16 a[236:237], v11 offset:12288\n"
    "ds_read_b64_tr_b16 a[238:239], v11 offset:13312\n"
    "buffer_load_dwordx4 v[188:191], v141, s[44:47], s89 offen offset:1024 sc1\n"
    "s_waitcnt lgkmcnt(12)\n"
    "v_mfma_f32_32x32x16_bf16 v[64:79], a[248:251], v[204:207], v[64:79]\n"
    "ds_read_b64_tr_b16 a[240:241], v10 offset:16384\n"
    "ds_read_b64_tr_b16 a[242:243], v10 offset:17408\n"
    "v_mfma_f32_32x32x16_bf16 v[80:95], a[252:255], v[204:207], v[80:95]\n"
    "ds_read_b64_tr_b16 a[244:245], v11 offset:16384\n"
    "ds_read_b64_tr_b16 a[246:247], v11 offset:17408\n"
    "buffer_load_dwordx4 v[192:195], v141, s[44:47], s90 offen offset:0 sc1\n"
    "s_waitcnt vmcnt(24)\n"
    "ds_read_b64_tr_b16 a[168:169], v136 offset:0\n"
    "s_waitcnt lgkmcnt(13)\n"
    "v_mfma_f32_32x32x16_bf16 v[64:79], a[224:227], v[208:211], v[64:79]\n"
    "ds_read_b64_tr_b16 a[248:249], v10 offset:20480\n"
    "ds_read_b64_tr_b16 a[250:251], v10 offset:21504\n"
    "buffer_load_dwordx4 v143, s[36:39], s5 offen offset:1024 lds\n"
    "ds_read_b64_tr_b16 a[170:171], v136 offset:1024\n"
    "ds_read_b64_tr_b16 a[172:173], v137 offset:0\n"
    "ds_read_b64_tr_b16 a[174:175], v137 offset:1024\n"
    "v_mfma_f32_32x32x16_bf16 v[80:95], a[228:231], v[208:211], v[80:95]\n"
    "ds_read_b64_tr_b16 a[252:253], v11 offset:20480\n"
    "ds_read_b64_tr_b16 a[254:255], v11 offset:21504\n"
    "buffer_load_dwordx4 v[196:199], v141, s[44:47], s90 offen offset:1024 sc1\n"
    "s_waitcnt lgkmcnt(12)\n"
    "v_mfma_f32_32x32x16_bf16 v[64:79], a[232:235], v[212:215], v[64:79]\n"
    "ds_read_b64_tr_b16 a[224:225], v10 offset:24576\n"
    "ds_read_b64_tr_b16 a[226:227], v10 offset:25600\n"
    "s_add_u32 s5, s5, 14336\n"
    "s_add_u32 m0, s75, 8192\n"
    "s_nop 0\n"
    "buffer_load_dwordx4 v4, s[36:39], s5 offen lds\n"
    "v_mfma_f32_32x32x16_bf16 v[80:95], a[236:239], v[212:215], v[80:95]\n"
    "ds_read_b64_tr_b16 a[228:229], v11 offset:24576\n"
    "ds_read_b64_tr_b16 a[230:231], v11 offset:25600\n"
    "s_waitcnt vmcnt(25)\n"
    "ds_read_b64_tr_b16 a[176:177], v136 offset:2048\n"
    "ds_read_b64_tr_b16 a[178:179], v136 offset:3072\n"
    "s_waitcnt lgkmcnt(14)\n"
    "v_mfma_f32_32x32x16_bf16 v[64:79], a[240:243], v[216:219], v[64:79]\n"
    "ds_read_b64_tr_b16 a[232:233], v10 offset:28672\n"
    "ds_read_b64_tr_b16 a[234:235], v10 offset:29696\n"
    "buffer_load_dwordx4 v143, s[36:39], s5 offen offset:1024 lds\n"
    "ds_read_b64_tr_b16 a[180:181], v137 offset:2048\n"
    "ds_read_b64_tr_b16 a[182:183], v137 offset:3072\n"
    "s_waitcnt vmcnt(21)\n"
    "v_mfma_f32_32x32x16_bf16 v[80:95], a[244:247], v[216:219], v[80:95]\n"
    "ds_read_b64_tr_b16 a[236:237], v11 offset:28672\n"
    "ds_read_b64_tr_b16 a[238:239], v11 offset:29696\n"
    "ds_read_b64_tr_b16 a[184:185], v136 offset:4096\n"
    "ds_read_b64_tr_b16 a[186:187], v136 offset:5120\n"
    "ds_read_b64_tr_b16 a[188:189], v137 offset:4096\n"
    "s_waitcnt lgkmcnt(14)\n"
    "v_mfma_f32_32x32x16_bf16 v[64:79], a[248:251], v[220:223], v[64:79]\n"
    "ds_read_b64_tr_b16 a[240:241], v10 offset:32768\n"
    "ds_read_b64_tr_b16 a[242:243], v10 offset:33792\n"
    "s_mul_i32 s5, s90, 14\n"
    "s_add_u32 s5, s5, 28672\n"
    "s_add_u32 m0, s75, 0\n"
    "s_nop 0\n"
    "buffer_load_dwordx4 v4, s[36:39], s5 offen lds\n"
    "ds_read_b64_tr_b16 a[190:191], v137 offset:5120\n"
    "v_mfma_f32_32x32x16_bf16 v[80:95], a[252:255], v[220:223], v[80:95]\n"
    "ds_read_b64_tr_b16 a[244:245], v11 offset:32768\n"
    "ds_read_b64_tr_b16 a[246:247], v11 offset:33792\n"
    "s_waitcnt lgkmcnt(14)\n"
    "v_mfma_f32_32x32x16_bf16 v[64:79], a[224:227], v[224:227], v[64:79]\n"
    "ds_read_b64_tr_b16 a[248:249], v10 offset:36864\n"
    "ds_read_b64_tr_b16 a[250:251], v10 offset:37888\n"
    "buffer_load_dwordx4 v143, s[36:39], s5 offen offset:1024 lds\n"
    "v_mfma_f32_32x32x16_bf16 v[80:95], a[228:231], v[224:227], v[80:95]\n"
    "ds_read_b64_tr_b16 a[252:253], v11 offset:36864\n"
    "ds_read_b64_tr_b16 a[254:255], v11 offset:37888\n"
    "s_waitcnt lgkmcnt(12)\n"
    "v_mfma_f32_32x32x16_bf16 v[64:79], a[232:235], v[228:231], v[64:79]\n"
    "ds_read_b64_tr_b16 a[224:225], v10 offset:40960\n"
    "ds_read_b64_tr_b16 a[226:227], v10 offset:41984\n"
    "v_mfma_f32_32x32x16_bf16 v[64:79], v[100:103], v[108:111], v[64:79]\n"
    "s_add_u32 s5, s5, 14336\n"
    "s_add_u32 m0, s75, 2048\n"
    "s_nop 0\n"
    "buffer_load_dwordx4 v4, s[36:39], s5 offen lds\n"
    "v_mfma_f32_32x32x16_bf16 v[80:95], a[236:239], v[228:231], v[80:95]\n"
    "ds_read_b64_tr_b16 a[228:229], v11 offset:40960\n"
    "ds_read_b64_tr_b16 a[230:231], v11 offset:41984\n"
    "v_mfma_f32_32x32x16_bf16 v[80:95], v[102:105], v[108:111], v[80:95]\n"
    "s_waitcnt lgkmcnt(8)\n"
    "v_mfma_f32_32x32x16_bf16 v[64:79], a[240:243], v[232:235], v[64:79]\n"
    "ds_read_b64_tr_b16 a[232:233], v10 offset:45056\n"
    "ds_read_b64_tr_b16 a[234:235], v10 offset:46080\n"
    "buffer_load_dwordx4 v143, s[36:39], s5 offen offset:1024 lds\n"
    "v_mfma_f32_32x32x16_bf16 v[80:95], a[244:247], v[232:235], v[80:95]\n"
    "ds_read_b64_tr_b16 a[236:237], v11 offset:45056\n"
    "ds_read_b64_tr_b16 a[238:239], v11 offset:46080\n"
    "s_waitcnt lgkmcnt(8)\n"
    "v_mfma_f32_32x32x16_bf16 v[64:79], a[248:251], v[236:239], v[64:79]\n"
    "ds_read_b64_tr_b16 a[240:241], v10 offset:49152\n"
    "ds_read_b64_tr_b16 a[242:243], v10 offset:50176\n"
    "v_mfma_f32_32x32x16_bf16 v[80:95], a[252:255], v[236:239], v[80:95]\n"
    "ds_read_b64_tr_b16 a[244:245], v11 offset:49152\n"
    "ds_read_b64_tr_b16 a[246:247], v11 offset:50176\n"
    "s_waitcnt lgkmcnt(8)\n"
    "v_mfma_f32_32x32x16_bf16 v[64:79], a[224:227], v[240:243], v[64:79]\n"
    "ds_read_b64_tr_b16 a[248:249], v10 offset:53248\n"
    "ds_read_b64_tr_b16 a[250:251], v10 offset:54272\n"
    "s_waitcnt vmcnt(7)\n"
    "ds_read_b64_tr_b16 a[192:193], v136 offset:6144\n"
    "ds_read_b64_tr_b16 a[194:195], v136 offset:7168\n"
    "ds_read_b64_tr_b16 a[196:197], v137 offset:6144\n"
    "v_mfma_f32_32x32x16_bf16 v[80:95], a[228:231], v[240:243], v[80:95]\n"
    "ds_read_b64_tr_b16 a[252:253], v11 offset:53248\n"
    "ds_read_b64_tr_b16 a[254:255], v11 offset:54272\n"
    "ds_read_b64_tr_b16 a[198:199], v137 offset:7168\n"
    "s_waitcnt lgkmcnt(12)\n"
    "v_mfma_f32_32x32x16_bf16 v[64:79], a[232:235], v[244:247], v[64:79]\n"
    "v_cmp_ne_u32 vcc, s56, v29\n"
    "s_cbranch_vccnz L_retry_29_%=\n"
    "L_go_30_%=:\n"
    "buffer_load_dwordx4 v[200:203], v141, s[44:47], s84 offen offset:2048 sc1\n"
    "v_mfma_f32_32x32x16_bf16 v[80:95], a[236:239], v[244:247], v[80:95]\n"
    "s_waitcnt lgkmcnt(8)\n"
    "v_mfma_f32_32x32x16_bf16 v[64:79], a[240:243], v[248:251], v[64:79]\n"
    "buffer_load_dwordx4 v[204:207], v141, s[44:47], s84 offen offset:3072 sc1\n"
    "s_waitcnt vmcnt(6)\n"
    "ds_read_b64_tr_b16 a[200:201], v136 offset:8192\n"
    "ds_read_b64_tr_b16 a[202:203], v136 offset:9216\n"
    "ds_read_b64_tr_b16 a[204:205], v137 offset:8192\n"
    "ds_read_b64_tr_b16 a[206:207], v137 offset:9216\n"
    "v_mfma_f32_32x32x16_bf16 v[80:95], a[244:247], v[248:251], v[80:95]\n"
    "s_waitcnt lgkmcnt(5)\n"
    "v_mfma_f32_32x32x16_bf16 v[64:79], a[248:251], v[252:255], v[64:79]\n"
    "buffer_load_dwordx4 v[208:211], v141, s[44:47], s85 offen offset:2048 sc1\n"
    "v_mfma_f32_32x32x16_bf16 v[80:95], a[252:255], v[252:255], v[80:95]\n"
    "v_mfma_f32_32x32x16_bf16 v[32:47], a[0:3], v[144:147], 0\n"
    "buffer_load_dwordx4 v[212:215], v141, s[44:47], s85 offen offset:3072 sc1\n"
    "s_waitcnt vmcnt(6)\n"
    "ds_read_b64_tr_b16 a[208:209], v136 offset:0\n"
    "ds_read_b64_tr_b16 a[210:211], v136 offset:1024\n"
    "ds_read_b64_tr_b16 a[212:213], v137 offset:0\n"
    "ds_read_b64_tr_b16 a[214:215], v137 offset:1024\n"
    "v_mfma_f32_32x32x16_bf16 v[48:63], a[4:7], v[144:147], 0\n"
    "s_nop 1\n"
    "v_mul_f32 v64, v64, v21\n"
    "v_mul_f32 v64, v64, v72\n"
    "v_mul_f32 v65, v65, v21\n"
    "v_mul_f32 v65, v65, v73\n"
    "v_mfma_f32_32x32x16_bf16 v[32:47], a[8:11], v[148:151], v[32:47]\n"
    "v_cvt_pk_bf16_f32 v112, v64, v65\n"
    "v_mul_f32 v66, v66, v21\n"
    "v_mul_f32 v66, v66, v74\n"
    "v_mul_f32 v67, v67, v21\n"
    "v_mul_f32 v67, v67, v75\n"
    "buffer_load_dwordx4 v[216:219], v141, s[44:47], s86 offen offset:2048 sc1\n"
    "v_mfma_f32_32x32x16_bf16 v[48:63], a[12:15], v[148:151], v[48:63]\n"
    "v_cvt_pk_bf16_f32 v113, v66, v67\n"
    "v_mul_f32 v68, v68, v21\n"
    "v_mul_f32 v68, v68, v76\n"
    "v_mul_f32 v69, v69, v21\n"
    "v_mfma_f32_32x32x16_bf16 v[32:47], a[16:19], v[152:155], v[32:47]\n"
    "v_mul_f32 v69, v69, v77\n"
    "v_cvt_pk_bf16_f32 v114, v68, v69\n"
    "v_mul_f32 v70, v70, v21\n"
    "v_mul_f32 v70, v70, v78\n"
    "v_mul_f32 v71, v71, v21\n"
    "s_waitcnt vmcnt(5)\n"
    "v_mfma_f32_32x32x16_bf16 v[48:63], a[20:23], v[152:155], v[48:63]\n"
    "v_mul_f32 v71, v71, v79\n"
    "v_cvt_pk_bf16_f32 v115, v70, v71\n"
    "buffer_store_dwordx4 v[112:115], v1, s[44:47], s64 offen offset:2048 sc1\n"
    "ds_read_b64_tr_b16 a[216:217], v136 offset:2048\n"
    "v_mfma_f32_32x32x16_bf16 v[32:47], a[24:27], v[156:159], v[32:47]\n"
    "v_mul_f32 v80, v80, v21\n"
    "v_mul_f32 v80, v80, v88\n"
    "v_mul_f32 v81, v81, v21\n"
    "v_mul_f32 v81, v81, v89\n"
    "v_cvt_pk_bf16_f32 v116, v80, v81\n"
    "buffer_load_dwordx4 v[220:223], v141, s[44:47], s86 offen offset:3072 sc1\n"
    "v_mfma_f32_32x32x16_bf16 v[48:63], a[28:31], v[156:159], v[48:63]\n"
    "v_mul_f32 v82, v82, v21\n"
    "v_mul_f32 v82, v82, v90\n"
    "v_mul_f32 v83, v83, v21\n"
    "v_mul_f32 v83, v83, v91\n"
    "ds_read_b64_tr_b16 a[218:219], v136 offset:3072\n"
    "v_mfma_f32_32x32x16_bf16 v[32:47], a[32:35], v[160:163], v[32:47]\n"
    "v_cvt_pk_bf16_f32 v117, v82, v83\n"
    "v_mul_f32 v84, v84, v21\n"
    "v_mul_f32 v84, v84, v92\n"
    "v_mul_f32 v85, v85, v21\n"
    "v_mul_f32 v85, v85, v93\n"
    "buffer_load_dwordx4 v[224:227], v141, s[44:47], s87 offen offset:2048 sc1\n"
    "v_mfma_f32_32x32x16_bf16 v[48:63], a[36:39], v[160:163], v[48:63]\n"
    "v_cvt_pk_bf16_f32 v118, v84, v85\n"
    "v_mul_f32 v86, v86, v21\n"
    "v_mul_f32 v86, v86, v94\n"
    "v_mul_f32 v87, v87, v21\n"
    "ds_read_b64_tr_b16 a[220:221], v137 offset:2048\n"
    "v_mfma_f32_32x32x16_bf16 v[32:47], a[40:43], v[164:167], v[32:47]\n"
    "v_mul_f32 v87, v87, v95\n"
    "v_cvt_pk_bf16_f32 v119, v86, v87\n"
    "buffer_store_dwordx4 v[116:119], v1, s[44:47], s64 offen offset:3072 sc1\n"
    "ds_read_b64_tr_b16 a[222:223], v137 offset:3072\n"
    "v_mfma_f32_32x32x16_bf16 v[48:63], a[44:47], v[164:167], v[48:63]\n"
    "v_mfma_f32_32x32x16_bf16 v[32:47], a[48:51], v[168:171], v[32:47]\n"
    "buffer_load_dword v30, v12, s[44:47], s66 offen offset:896 sc1\n"
    "v_mfma_f32_32x32x16_bf16 v[48:63], a[52:55], v[168:171], v[48:63]\n"
    "v_mfma_f32_32x32x16_bf16 v[32:47], a[56:59], v[172:175], v[32:47]\n"
    "buffer_load_dwordx4 v[228:231], v141, s[44:47], s87 offen offset:3072 sc1\n"
    "v_mfma_f32_32x32x16_bf16 v[48:63], a[60:63], v[172:175], v[48:63]\n"
    "v_mfma_f32_32x32x16_bf16 v[32:47], a[64:67], v[176:179], v[32:47]\n"
    "buffer_load_dwordx4 v[232:235], v141, s[44:47], s88 offen offset:2048 sc1\n"
    "v_mfma_f32_32x32x16_bf16 v[48:63], a[68:71], v[176:179], v[48:63]\n"
    "v_mfma_f32_32x32x16_bf16 v[32:47], a[72:75], v[180:183], v[32:47]\n"
    "buffer_load_dwordx4 v[236:239], v141, s[44:47], s88 offen offset:3072 sc1\n"
    "v_mfma_f32_32x32x16_bf16 v[48:63], a[76:79], v[180:183], v[48:63]\n"
    "v_mfma_f32_32x32x16_bf16 v[32:47], a[80:83], v[184:187], v[32:47]\n"
    "buffer_load_dwordx4 v[240:243], v141, s[44:47], s89 offen offset:2048 sc1\n"
    "v_mfma_f32_32x32x16_bf16 v[48:63], a[84:87], v[184:187], v[48:63]\n"
    "v_mfma_f32_32x32x16_bf16 v[32:47], a[88:91], v[188:191], v[32:47]\n"
    "v_mfma_f32_32x32x16_bf16 v[48:63], a[92:95], v[188:191], v[48:63]\n"
    "buffer_load_dwordx4 v[244:247], v141, s[44:47], s89 offen offset:3072 sc1\n"
    "v_mfma_f32_32x32x16_bf16 v[32:47], a[96:99], v[192:195], v[32:47]\n"
    "v_mfma_f32_32x32x16_bf16 v[48:63], a[100:103], v[192:195], v[48:63]\n"
    "buffer_load_dwordx4 v[248:251], v141, s[44:47], s90 offen offset:2048 sc1\n"
    "v_mfma_f32_32x32x16_bf16 v[32:47], a[104:107], v[196:199], v[32:47]\n"
    "v_mfma_f32_32x32x16_bf16 v[48:63], a[108:111], v[196:199], v[48:63]\n"
    "buffer_load_dwordx4 v[252:255], v141, s[44:47], s90 offen offset:3072 sc1\n"
    "s_waitcnt vmcnt(16)\n"
    "v_mfma_f32_32x32x16_bf16 v[32:47], a[112:115], v[200:203], v[32:47]\n"
    "s_waitcnt vmcnt(7)\n"
    "v_cmp_eq_u32 vcc, -1, v30\n"
    "s_cbranch_vccnz L_retry_33_%=\n"
    "L_go_34_%=:\n"
    "v_mfma_f32_32x32x16_bf16 v[48:63], a[116:119], v[200:203], v[48:63]\n"
    "buffer_load_dwordx4 v[144:147], v142, s[44:47], s84 offen offset:0 sc1\n"
    "v_mfma_f32_32x32x16_bf16 v[32:47], a[120:123], v[204:207], v[32:47]\n"
    "v_mfma_f32_32x32x16_bf16 v[48:63], a[124:127], v[204:207], v[48:63]\n"
    "buffer_load_dwordx4 v[148:151], v142, s[44:47], s84 offen offset:1024 sc1\n"
    "v_mfma_f32_32x32x16_bf16 v[32:47], a[128:131], v[208:211], v[32:47]\n"
    "s_sub_u32 s71, s75, 57344\n"
    "v_add_u32 v8, s71, v1\n"
    "v_mfma_f32_32x32x16_bf16 v[48:63], a[132:135], v[208:211], v[48:63]\n"
    "buffer_load_dwordx4 v[152:155], v142, s[44:47], s85 offen offset:0 sc1\n"
    "v_mfma_f32_32x32x16_bf16 v[32:47], a[136:139], v[212:215], v[32:47]\n"
    "s_xor_b32 s71, s48, 1\n"
    "s_mul_i32 s71, s71, 10240\n"
    "s_add_u32 s71, s71, 57344\n"
    "v_add_u32 v9, s71, v1\n"
    "v_mfma_f32_32x32x16_bf16 v[48:63], a[140:143], v[212:215], v[48:63]\n"
    "buffer_load_dwordx4 v[156:159], v142, s[44:47], s85 offen offset:1024 sc1\n"
    "v_mfma_f32_32x32x16_bf16 v[32:47], a[144:147], v[216:219], v[32:47]\n"
    "s_xor_b32 s71, s48, 2\n"
    "s_mul_i32 s71, s71, 10240\n"
    "s_add_u32 s71, s71, 59392\n"
    "v_add_u32 v10, s71, v1\n"
    "v_mfma_f32_32x32x16_bf16 v[48:63], a[148:151], v[216:219], v[48:63]\n"
    "buffer_load_dwordx4 v[160:163], v142, s[44:47], s86 offen offset:0 sc1\n"
    "v_mfma_f32_32x32x16_bf16 v[32:47], a[152:155], v[220:223], v[32:47]\n"
    "s_xor_b32 s71, s48, 3\n"
    "s_mul_i32 s71, s71, 10240\n"
    "s_add_u32 s71, s71, 61440\n"
    "v_add_u32 v11, s71, v1\n"
    "v_mfma_f32_32x32x16_bf16 v[48:63], a[156:159], v[220:223], v[48:63]\n"
    "buffer_load_dwordx4 v[164:167], v142, s[44:47], s86 offen offset:1024 sc1\n"
    "v_mfma_f32_32x32x16_bf16 v[32:47], a[160:163], v[224:227], v[32:47]\n"
    "v_mfma_f32_32x32x16_bf16 v[48:63], a[164:167], v[224:227], v[48:63]\n"
    "buffer_load_dwordx4 v[168:171], v142, s[44:47], s87 offen offset:0 sc1\n"
    "s_waitcnt vmcnt(13)\n"
    "v_mfma_f32_32x32x16_bf16 v[32:47], a[168:171], v[228:231], v[32:47]\n"
    "v_mfma_f32_32x32x16_bf16 v[48:63], a[172:175], v[228:231], v[48:63]\n"
    "buffer_load_dwordx4 v[172:175], v142, s[44:47], s87 offen offset:1024 sc1\n"
    "s_waitcnt vmcnt(13)\n"
    "v_mfma_f32_32x32x16_bf16 v[32:47], a[176:179], v[232:235], v[32:47]\n"
    "v_mfma_f32_32x32x16_bf16 v[48:63], a[180:183], v[232:235], v[48:63]\n"
    "buffer_load_dwordx4 v[176:179], v142, s[44:47], s88 offen offset:0 sc1\n"
    "s_waitcnt vmcnt(13)\n"
    "v_mfma_f32_32x32x16_bf16 v[32:47], a[184:187], v[236:239], v[32:47]\n"
    "v_mfma_f32_32x32x16_bf16 v[48:63], a[188:191], v[236:239], v[48:63]\n"
    "buffer_load_dwordx4 v[180:183], v142, s[44:47], s88 offen offset:1024 sc1\n"
    "s_waitcnt vmcnt(13)\n"
    "v_mfma_f32_32x32x16_bf16 v[32:47], a[192:195], v[240:243], v[32:47]\n"
    "s_waitcnt lgkmcnt(12)\n"
    "v_mfma_f32_32x32x16_bf16 v[48:63], a[196:199], v[240:243], v[48:63]\n"
    "buffer_load_dwordx4 v[184:187], v142, s[44:47], s89 offen offset:0 sc1\n"
    "s_waitcnt vmcnt(13) lgkmcnt(10)\n"
    "v_mfma_f32_32x32x16_bf16 v[32:47], a[200:203], v[244:247], v[32:47]\n"
    "s_waitcnt lgkmcnt(8)\n"
    "v_mfma_f32_32x32x16_bf16 v[48:63], a[204:207], v[244:247], v[48:63]\n"
    "buffer_load_dwordx4 v[188:191], v142, s[44:47], s89 offen offset:1024 sc1\n"
    "s_waitcnt vmcnt(13) lgkmcnt(6)\n"
    "v_mfma_f32_32x32x16_bf16 v[32:47], a[208:211], v[248:251], v[32:47]\n"
    "s_waitcnt lgkmcnt(4)\n"
    "v_mfma_f32_32x32x16_bf16 v[48:63], a[212:215], v[248:251], v[48:63]\n"
    "buffer_load_dwordx4 v[192:195], v142, s[44:47], s90 offen offset:0 sc1\n"
    "s_waitcnt vmcnt(13) lgkmcnt(2)\n"
    "v_mfma_f32_32x32x16_bf16 v[32:47], a[216:219], v[252:255], v[32:47]\n"
    "s_waitcnt lgkmcnt(0)\n"
    "v_mfma_f32_32x32x16_bf16 v[48:63], a[220:223], v[252:255], v[48:63]\n"
    "buffer_load_dwordx4 v[196:199], v142, s[44:47], s90 offen offset:1024 sc1\n"
    "s_waitcnt vmcnt(13)\n"
    "v_mfma_f32_32x32x16_bf16 v[64:79], a[0:3], v[144:147], 0\n"
    "v_mfma_f32_32x32x16_bf16 v[80:95], a[4:7], v[144:147], 0\n"
    "s_waitcnt vmcnt(12)\n"
    "v_mfma_f32_32x32x16_bf16 v[64:79], a[8:11], v[148:151], v[64:79]\n"
    "s_nop 3\n"
    "ds_write_b128 v8, v[40:43] offset:57344\n"
    "v_mfma_f32_32x32x16_bf16 v[80:95], a[12:15], v[148:151], v[80:95]\n"
    "ds_write_b128 v8, v[44:47] offset:58368\n"
    "s_waitcnt vmcnt(11)\n"
    "v_mfma_f32_32x32x16_bf16 v[64:79], a[16:19], v[152:155], v[64:79]\n"
    "buffer_load_dword v28, v12, s[44:47], s66 offen offset:2944 sc1\n"
    "ds_write_b128 v8, v[48:51] offset:59392\n"
    "v_mfma_f32_32x32x16_bf16 v[80:95], a[20:23], v[152:155], v[80:95]\n"
    "ds_write_b128 v8, v[52:55] offset:60416\n"
    "s_waitcnt vmcnt(11)\n"
    "v_mfma_f32_32x32x16_bf16 v[64:79], a[24:27], v[156:159], v[64:79]\n"
    "ds_write_b128 v8, v[56:59] offset:61440\n"
    "v_mfma_f32_32x32x16_bf16 v[80:95], a[28:31], v[156:159], v[80:95]\n"
    "buffer_load_dwordx4 v[200:203], v142, s[44:47], s84 offen offset:2048 sc1\n"
    "ds_write_b128 v8, v[60:63] offset:62464\n"
    "s_waitcnt vmcnt(11)\n"
    "v_mfma_f32_32x32x16_bf16 v[64:79], a[32:35], v[160:163], v[64:79]\n"
    "v_mfma_f32_32x32x16_bf16 v[80:95], a[36:39], v[160:163], v[80:95]\n"
    "buffer_load_dwordx4 v[204:207], v142, s[44:47], s84 offen offset:3072 sc1\n"
    "s_waitcnt vmcnt(11)\n"
    "v_mfma_f32_32x32x16_bf16 v[64:79], a[40:43], v[164:167], v[64:79]\n"
    "v_mfma_f32_32x32x16_bf16 v[80:95], a[44:47], v[164:167], v[80:95]\n"
    "buffer_load_dwordx4 v[208:211], v142, s[44:47], s85 offen offset:2048 sc1\n"
    "s_waitcnt vmcnt(11)\n"
    "v_mfma_f32_32x32x16_bf16 v[64:79], a[48:51], v[168:171], v[64:79]\n"
    "v_mfma_f32_32x32x16_bf16 v[80:95], a[52:55], v[168:171], v[80:95]\n"
    "buffer_load_dwordx4 v[212:215], v142, s[44:47], s85 offen offset:3072 sc1\n"
    "s_waitcnt vmcnt(11)\n"
    "v_mfma_f32_32x32x16_bf16 v[64:79], a[56:59], v[172:175], v[64:79]\n"
    "v_mfma_f32_32x32x16_bf16 v[80:95], a[60:63], v[172:175], v[80:95]\n"
    "buffer_load_dwordx4 v[216:219], v142, s[44:47], s86 offen offset:2048 sc1\n"
    "s_waitcnt vmcnt(11)\n"
    "v_mfma_f32_32x32x16_bf16 v[64:79], a[64:67], v[176:179], v[64:79]\n"
    "v_mfma_f32_32x32x16_bf16 v[80:95], a[68:71], v[176:179], v[80:95]\n"
    "buffer_load_dwordx4 v[220:223], v142, s[44:47], s86 offen offset:3072 sc1\n"
    "s_waitcnt vmcnt(11)\n"
    "v_mfma_f32_32x32x16_bf16 v[64:79], a[72:75], v[180:183], v[64:79]\n"
    "v_mfma_f32_32x32x16_bf16 v[80:95], a[76:79], v[180:183], v[80:95]\n"
    "buffer_load_dwordx4 v[224:227], v142, s[44:47], s87 offen offset:2048 sc1\n"
    "s_waitcnt vmcnt(11)\n"
    "v_mfma_f32_32x32x16_bf16 v[64:79], a[80:83], v[184:187], v[64:79]\n"
    "v_mfma_f32_32x32x16_bf16 v[80:95], a[84:87], v[184:187], v[80:95]\n"
    "buffer_load_dwordx4 v[228:231], v142, s[44:47], s87 offen offset:3072 sc1\n"
    "s_waitcnt vmcnt(11)\n"
    "v_mfma_f32_32x32x16_bf16 v[64:79], a[88:91], v[188:191], v[64:79]\n"
    "v_mfma_f32_32x32x16_bf16 v[80:95], a[92:95], v[188:191], v[80:95]\n"
    "buffer_load_dwordx4 v[232:235], v142, s[44:47], s88 offen offset:2048 sc1\n"
    "s_waitcnt vmcnt(11)\n"
    "v_mfma_f32_32x32x16_bf16 v[64:79], a[96:99], v[192:195], v[64:79]\n"
    "s_waitcnt vmcnt(9)\n"
    "v_cmp_eq_u32 vcc, -1, v28\n"
    "s_cbranch_vccnz L_retry_37_%=\n"
    "L_go_38_%=:\n"
    "v_mfma_f32_32x32x16_bf16 v[80:95], a[100:103], v[192:195], v[80:95]\n"
    "buffer_load_dwordx4 v[236:239], v142, s[44:47], s88 offen offset:3072 sc1\n"
    "v_mfma_f32_32x32x16_bf16 v[64:79], a[104:107], v[196:199], v[64:79]\n"
    "v_mfma_f32_32x32x16_bf16 v[80:95], a[108:111], v[196:199], v[80:95]\n"
    "buffer_load_dwordx4 v[240:243], v142, s[44:47], s89 offen offset:2048 sc1\n"
    "s_waitcnt vmcnt(10)\n"
    "v_mfma_f32_32x32x16_bf16 v[64:79], a[112:115], v[200:203], v[64:79]\n"
    "v_mfma_f32_32x32x16_bf16 v[80:95], a[116:119], v[200:203], v[80:95]\n"
    "buffer_load_dwordx4 v[244:247], v142, s[44:47], s89 offen offset:3072 sc1\n"
    "s_waitcnt vmcnt(10)\n"
    "v_mfma_f32_32x32x16_bf16 v[64:79], a[120:123], v[204:207], v[64:79]\n"
    "v_mfma_f32_32x32x16_bf16 v[80:95], a[124:127], v[204:207], v[80:95]\n"
    "buffer_load_dwordx4 v[248:251], v142, s[44:47], s90 offen offset:2048 sc1\n"
    "s_waitcnt vmcnt(10)\n"
    "v_mfma_f32_32x32x16_bf16 v[64:79], a[128:131], v[208:211], v[64:79]\n"
    "s_waitcnt lgkmcnt(0)\n"
    "s_barrier\n"
    "v_mfma_f32_32x32x16_bf16 v[80:95], a[132:135], v[208:211], v[80:95]\n"
    "buffer_load_dwordx4 v[252:255], v142, s[44:47], s90 offen offset:3072 sc1\n"
    "ds_read_b128 v[96:99], v9 offset:57344\n"
    "ds_read_b128 v[100:103], v9 offset:58368\n"
    "s_waitcnt vmcnt(10)\n"
    "v_mfma_f32_32x32x16_bf16 v[64:79], a[136:139], v[212:215], v[64:79]\n"
    "ds_read_b128 v[104:107], v10 offset:57344\n"
    "ds_read_b128 v[108:111], v10 offset:58368\n"
    "v_mfma_f32_32x32x16_bf16 v[80:95], a[140:143], v[212:215], v[80:95]\n"
    "ds_read_b128 v[112:115], v11 offset:57344\n"
    "ds_read_b128 v[116:119], v11 offset:58368\n"
    "s_waitcnt vmcnt(9)\n"
    "v_mfma_f32_32x32x16_bf16 v[64:79], a[144:147], v[216:219], v[64:79]\n"
    "v_mfma_f32_32x32x16_bf16 v[80:95], a[148:151], v[216:219], v[80:95]\n"
    "s_waitcnt vmcnt(8)\n"
    "v_mfma_f32_32x32x16_bf16 v[64:79], a[152:155], v[220:223], v[64:79]\n"
    "v_lshlrev_b32 v128, 16, v120\n"
    "v_and_b32 v129, 0xffff0000, v120\n"
    "v_lshlrev_b32 v130, 16, v121\n"
    "v_mfma_f32_32x32x16_bf16 v[80:95], a[156:159], v[220:223], v[80:95]\n"
    "v_and_b32 v131, 0xffff0000, v121\n"
    "v_lshlrev_b32 v132, 16, v122\n"
    "v_and_b32 v133, 0xffff0000, v122\n"
    "s_waitcnt vmcnt(7)\n"
    "v_mfma_f32_32x32x16_bf16 v[64:79], a[160:163], v[224:227], v[64:79]\n"
    "v_lshlrev_b32 v134, 16, v123\n"
    "v_and_b32 v135, 0xffff0000, v123\n"
    "v_add_f32 v32, v32, v128\n"
    "v_mfma_f32_32x32x16_bf16 v[80:95], a[164:167], v[224:227], v[80:95]\n"
    "v_add_f32 v33, v33, v129\n"
    "v_add_f32 v34, v34, v130\n"
    "v_add_f32 v35, v35, v131\n"
    "s_waitcnt vmcnt(6)\n"
    "v_mfma_f32_32x32x16_bf16 v[64:79], a[168:171], v[228:231], v[64:79]\n"
    "v_add_f32 v36, v36, v132\n"
    "v_add_f32 v37, v37, v133\n"
    "v_add_f32 v38, v38, v134\n"
    "v_mfma_f32_32x32x16_bf16 v[80:95], a[172:175], v[228:231], v[80:95]\n"
    "v_add_f32 v39, v39, v135\n"
    "s_waitcnt lgkmcnt(5)\n"
    "v_add_f32 v32, v32, v96\n"
    "v_add_f32 v33, v33, v97\n"
    "s_waitcnt vmcnt(5)\n"
    "v_mfma_f32_32x32x16_bf16 v[64:79], a[176:179], v[232:235], v[64:79]\n"
    "v_add_f32 v34, v34, v98\n"
    "v_add_f32 v35, v35, v99\n"
    "s_waitcnt lgkmcnt(4)\n"
    "v_add_f32 v36, v36, v100\n"
    "v_mfma_f32_32x32x16_bf16 v[80:95], a[180:183], v[232:235], v[80:95]\n"
    "v_add_f32 v37, v37, v101\n"
    "v_add_f32 v38, v38, v102\n"
    "v_add_f32 v39, v39, v103\n"
    "s_waitcnt vmcnt(4)\n"
    "v_mfma_f32_32x32x16_bf16 v[80:95], a[188:191], v[236:239], v[80:95]\n"
    "s_waitcnt lgkmcnt(3)\n"
    "v_add_f32 v32, v32, v104\n"
    "v_add_f32 v33, v33, v105\n"
    "v_add_f32 v34, v34, v106\n"
    "s_waitcnt vmcnt(3)\n"
    "v_mfma_f32_32x32x16_bf16 v[80:95], a[196:199], v[240:243], v[80:95]\n"
    "v_add_f32 v35, v35, v107\n"
    "s_waitcnt lgkmcnt(2)\n"
    "v_add_f32 v36, v36, v108\n"
    "v_add_f32 v37, v37, v109\n"
    "s_waitcnt vmcnt(2)\n"
    "v_mfma_f32_32x32x16_bf16 v[80:95], a[204:207], v[244:247], v[80:95]\n"
    "v_add_f32 v38, v38, v110\n"
    "v_add_f32 v39, v39, v111\n"
    "s_waitcnt lgkmcnt(1)\n"
    "v_add_f32 v32, v32, v112\n"
    "s_waitcnt vmcnt(1)\n"
    "v_mfma_f32_32x32x16_bf16 v[80:95], a[212:215], v[248:251], v[80:95]\n"
    "v_add_f32 v33, v33, v113\n"
    "v_add_f32 v34, v34, v114\n"
    "v_add_f32 v35, v35, v115\n"
    "s_waitcnt vmcnt(0)\n"
    "v_mfma_f32_32x32x16_bf16 v[80:95], a[220:223], v[252:255], v[80:95]\n"
    "s_waitcnt lgkmcnt(0)\n"
    "v_add_f32 v36, v36, v116\n"
    "v_add_f32 v37, v37, v117\n"
    "v_add_f32 v38, v38, v118\n"
    "v_mfma_f32_32x32x16_bf16 v[64:79], a[184:187], v[236:239], v[64:79]\n"
    "v_add_f32 v39, v39, v119\n"
    "v_cvt_pk_bf16_f32 v128, v32, v33\n"
    "v_cvt_pk_bf16_f32 v129, v34, v35\n"
    "v_mfma_f32_32x32x16_bf16 v[64:79], a[192:195], v[240:243], v[64:79]\n"
    "v_cvt_pk_bf16_f32 v130, v36, v37\n"
    "v_cvt_pk_bf16_f32 v131, v38, v39\n"
    "s_nop 0\n"
    "v_permlane32_swap_b32 v128, v130\n"
    "ds_write_b128 v8, v[80:83] offset:2048\n"
    "v_mfma_f32_32x32x16_bf16 v[64:79], a[200:203], v[244:247], v[64:79]\n"
    "v_permlane32_swap_b32 v129, v131\n"
    "buffer_store_dwordx4 v[128:131], v15, s[40:43], s69 offen\n"
    "ds_write_b128 v8, v[84:87] offset:3072\n"
    "v_mfma_f32_32x32x16_bf16 v[64:79], a[208:211], v[248:251], v[64:79]\n"
    "v_lshlrev_b32 v128, 16, v124\n"
    "v_and_b32 v129, 0xffff0000, v124\n"
    "v_lshlrev_b32 v130, 16, v125\n"
    "v_and_b32 v131, 0xffff0000, v125\n"
    "ds_write_b128 v8, v[88:91] offset:4096\n"
    "v_mfma_f32_32x32x16_bf16 v[64:79], a[216:219], v[252:255], v[64:79]\n"
    "v_lshlrev_b32 v132, 16, v126\n"
    "v_and_b32 v133, 0xffff0000, v126\n"
    "v_lshlrev_b32 v134, 16, v127\n"
    "v_and_b32 v135, 0xffff0000, v127\n"
    "ds_write_b128 v8, v[92:95] offset:5120\n"
    "s_nop 6\n"
    "ds_write_b128 v8, v[72:75] offset:0\n"
    "ds_write_b128 v8, v[76:79] offset:1024\n"
    "v_cmp_u_f32 vcc, v64, v64\n"
    "s_cbranch_vccnz L_redo_41_%=\n"
    "L_go_42_%=:\n"
    "s_waitcnt lgkmcnt(0)\n"
    "s_barrier\n"
    "ds_read_b128 v[96:99], v9 offset:0\n"
    "ds_read_b128 v[100:103], v9 offset:1024\n"
    "ds_read_b128 v[104:107], v10 offset:0\n"
    "ds_read_b128 v[108:111], v10 offset:1024\n"
    "ds_read_b128 v[112:115], v11 offset:0\n"
    "ds_read_b128 v[116:119], v11 offset:1024\n"
    "v_pk_add_f32 v[64:65], v[64:65], v[128:129]\n"
    "v_pk_add_f32 v[66:67], v[66:67], v[130:131]\n"
    "v_pk_add_f32 v[68:69], v[68:69], v[132:133]\n"
    "v_pk_add_f32 v[70:71], v[70:71], v[134:135]\n"
    "s_waitcnt lgkmcnt(5)\n"
    "v_pk_add_f32 v[64:65], v[64:65], v[96:97]\n"
    "v_pk_add_f32 v[66:67], v[66:67], v[98:99]\n"
    "s_waitcnt lgkmcnt(4)\n"
    "v_pk_add_f32 v[68:69], v[68:69], v[100:101]\n"
    "v_pk_add_f32 v[70:71], v[70:71], v[102:103]\n"
    "s_waitcnt lgkmcnt(3)\n"
    "v_pk_add_f32 v[64:65], v[64:65], v[104:105]\n"
    "v_pk_add_f32 v[66:67], v[66:67], v[106:107]\n"
    "s_waitcnt lgkmcnt(2)\n"
    "v_pk_add_f32 v[68:69], v[68:69], v[108:109]\n"
    "v_pk_add_f32 v[70:71], v[70:71], v[110:111]\n"
    "s_waitcnt lgkmcnt(1)\n"
    "v_pk_add_f32 v[64:65], v[64:65], v[112:113]\n"
    "v_pk_add_f32 v[66:67], v[66:67], v[114:115]\n"
    "s_waitcnt lgkmcnt(0)\n"
    "v_pk_add_f32 v[68:69], v[68:69], v[116:117]\n"
    "v_pk_add_f32 v[70:71], v[70:71], v[118:119]\n"
    "v_cvt_pk_bf16_f32 v128, v64, v65\n"
    "v_cvt_pk_bf16_f32 v129, v66, v67\n"
    "v_cvt_pk_bf16_f32 v130, v68, v69\n"
    "v_cvt_pk_bf16_f32 v131, v70, v71\n"
    "s_nop 0\n"
    "v_permlane32_swap_b32 v128, v130\n"
    "v_permlane32_swap_b32 v129, v131\n"
    "buffer_store_dwordx4 v[128:131], v15, s[40:43], s70 offen\n"
    "s_branch L_fin_%=\n"
    "L_idle_%=:\n"
    "s_sub_u32 s60, s55, 28\n"
    "s_lshl_b32 s60, s60, 2\n"
    "s_add_u32 s60, s60, s48\n"
    "v_lshrrev_b32 v28, 3, v22\n"
    "v_min_u32 v28, 6, v28\n"
    "v_lshlrev_b32 v28, 12, v28\n"
    "v_and_b32 v29, 7, v22\n"
    "v_lshl_add_u32 v28, v29, 7, v28\n"
    "s_and_b32 s61, %1, 7\n"
    "s_lshl_b32 s61, s61, 4\n"
    "s_add_u32 s61, s61, s60\n"
    "v_lshlrev_b32 v30, 7, v22\n"
    "v_mov_b32 v31, 2147483392\n"
    "s_lshl_b32 s62, s61, 8\n"
    "s_add_u32 s62, s62, 62464\n"
    "v_add_u32 v29, s62, v30\n"
    "v_cmp_gt_u32 vcc, 26, v22\n"
    "s_nop 1\n"
    "v_cndmask_b32 v31, v31, v29, vcc\n"
    "s_lshl_b32 s62, s61, 10\n"
    "s_add_u32 s62, s62, 129024\n"
    "v_add_u32 v29, s62, v30\n"
    "v_cmp_gt_u32 vcc, 24, v22\n"
    "s_nop 1\n"
    "v_cndmask_b32 v31, v31, v29, vcc\n"
    "s_lshl_b32 s62, s61, 11\n"
    "s_add_u32 s62, s62, 262144\n"
    "v_add_u32 v29, s62, v30\n"
    "v_cmp_gt_u32 vcc, 16, v22\n"
    "s_nop 1\n"
    "v_cndmask_b32 v31, v31, v29, vcc\n"
    "buffer_load_dword v39, v31, s[44:47], 0 offen sc1\n"
    "v_and_b32 v28, 7, v22\n"
    "v_min_u32 v28, 6, v28\n"
    "v_lshlrev_b32 v28, 7, v28\n"
    "v_lshrrev_b32 v29, 3, v22\n"
    "v_mul_u32_u24 v29, 896, v29\n"
    "v_add_u32 v28, v28, v29\n"
    "s_and_b32 s61, s60, 3\n"
    "s_mul_i32 s61, s61, 448\n"
    "s_lshr_b32 s62, s60, 2\n"
    "s_lshl_b32 s62, s62, 3\n"
    "s_add_u32 s61, s61, s62\n"
    "s_mul_i32 s61, s61, 896\n"
    "s_and_b32 s63, %1, 7\n"
    "s_cmp_eq_u32 s63, 7\n"
    "s_cselect_b32 s63, 3, s63\n"
    "s_mul_i32 s63, s63, 57344\n"
    "s_add_u32 s62, s61, s63\n"
    "buffer_load_dword v40, v28, s[36:39], s62 offen\n"
    "s_add_u32 s63, s63, 57344\n"
    "s_cmp_eq_u32 s63, 401408\n"
    "s_cselect_b32 s63, 0, s63\n"
    "s_add_u32 s62, s61, s63\n"
    "buffer_load_dword v41, v28, s[36:39], s62 offen\n"
    "s_add_u32 s63, s63, 57344\n"
    "s_cmp_eq_u32 s63, 401408\n"
    "s_cselect_b32 s63, 0, s63\n"
    "s_add_u32 s62, s61, s63\n"
    "buffer_load_dword v42, v28, s[36:39], s62 offen\n"
    "s_add_u32 s63, s63, 57344\n"
    "s_cmp_eq_u32 s63, 401408\n"
    "s_cselect_b32 s63, 0, s63\n"
    "s_add_u32 s62, s61, s63\n"
    "buffer_load_dword v43, v28, s[36:39], s62 offen\n"
    "s_add_u32 s63, s63, 57344\n"
    "s_cmp_eq_u32 s63, 401408\n"
    "s_cselect_b32 s63, 0, s63\n"
    "s_add_u32 s62, s61, s63\n"
    "buffer_load_dword v44, v28, s[36:39], s62 offen\n"
    "s_add_u32 s63, s63, 57344\n"
    "s_cmp_eq_u32 s63, 401408\n"
    "s_cselect_b32 s63, 0, s63\n"
    "s_add_u32 s62, s61, s63\n"
    "buffer_load_dword v45, v28, s[36:39], s62 offen\n"
    "s_add_u32 s63, s63, 57344\n"
    "s_cmp_eq_u32 s63, 401408\n"
    "s_cselect_b32 s63, 0, s63\n"
    "s_add_u32 s62, s61, s63\n"
    "buffer_load_dword v46, v28, s[36:39], s62 offen\n"
    "s_add_u32 s63, s63, 57344\n"
    "s_cmp_eq_u32 s63, 401408\n"
    "s_cselect_b32 s63, 0, s63\n"
    "s_add_u32 s61, s61, 28672\n"
    "s_add_u32 s62, s61, s63\n"
    "buffer_load_dword v47, v28, s[36:39], s62 offen\n"
    "s_add_u32 s63, s63, 57344\n"
    "s_cmp_eq_u32 s63, 401408\n"
    "s_cselect_b32 s63, 0, s63\n"
    "s_add_u32 s62, s61, s63\n"
    "buffer_load_dword v48, v28, s[36:39], s62 offen\n"
    "s_add_u32 s63, s63, 57344\n"
    "s_cmp_eq_u32 s63, 401408\n"
    "s_cselect_b32 s63, 0, s63\n"
    "s_add_u32 s62, s61, s63\n"
    "buffer_load_dword v49, v28, s[36:39], s62 offen\n"
    "s_add_u32 s63, s63, 57344\n"
    "s_cmp_eq_u32 s63, 401408\n"
    "s_cselect_b32 s63, 0, s63\n"
    "s_add_u32 s62, s61, s63\n"
    "buffer_load_dword v50, v28, s[36:39], s62 offen\n"
    "s_add_u32 s63, s63, 57344\n"
    "s_cmp_eq_u32 s63, 401408\n"
    "s_cselect_b32 s63, 0, s63\n"
    "s_add_u32 s62, s61, s63\n"
    "buffer_load_dword v51, v28, s[36:39], s62 offen\n"
    "s_add_u32 s63, s63, 57344\n"
    "s_cmp_eq_u32 s63, 401408\n"
    "s_cselect_b32 s63, 0, s63\n"
    "s_add_u32 s62, s61, s63\n"
    "buffer_load_dword v52, v28, s[36:39], s62 offen\n"
    "s_add_u32 s63, s63, 57344\n"
    "s_cmp_eq_u32 s63, 401408\n"
    "s_cselect_b32 s63, 0, s63\n"
    "s_add_u32 s62, s61, s63\n"
    "buffer_load_dword v53, v28, s[36:39], s62 offen\n"
    "s_add_u32 s63, s63, 57344\n"
    "s_cmp_eq_u32 s63, 401408\n"
    "s_cselect_b32 s63, 0, s63\n"
    "s_add_u32 s61, s61, 28672\n"
    "L_fin_%=:\n"
    "s_waitcnt vmcnt(0)\n"
    "s_endpgm\n"
    "L_end_%=:\n"
    "L_sretry_7_%=:\n"
    "s_add_u32 s82, s82, 1\n"
    "s_cmp_lt_u32 s82, 4096\n"
    "s_cbranch_scc0 L_sgo_8_%=\n"
    "buffer_load_dwordx4 v[80:83], v10, s[44:47], s60 offen offset:0 sc1\n"
    "buffer_load_dwordx4 v[84:87], v10, s[44:47], s60 offen offset:1024 sc1\n"
    "buffer_load_dwordx4 v[88:91], v10, s[44:47], s60 offen offset:2048 sc1\n"
    "buffer_load_dwordx4 v[92:95], v10, s[44:47], s60 offen offset:3072 sc1\n"
    "buffer_load_dwordx4 v[96:99], v10, s[44:47], s61 offen offset:0 sc1\n"
    "buffer_load_dwordx4 v[100:103], v10, s[44:47], s61 offen offset:1024 sc1\n"
    "buffer_load_dwordx4 v[104:107], v10, s[44:47], s61 offen offset:2048 sc1\n"
    "buffer_load_dwordx4 v[108:111], v10, s[44:47], s61 offen offset:3072 sc1\n"
    "buffer_load_dwordx4 v[112:115], v10, s[44:47], s62 offen offset:0 sc1\n"
    "buffer_load_dwordx4 v[116:119], v10, s[44:47], s62 offen offset:1024 sc1\n"
    "buffer_load_dwordx4 v[120:123], v10, s[44:47], s62 offen offset:2048 sc1\n"
    "buffer_load_dwordx4 v[124:127], v10, s[44:47], s62 offen offset:3072 sc1\n"
    "buffer_load_dwordx4 v[128:131], v10, s[44:47], s63 offen offset:0 sc1\n"
    "buffer_load_dwordx4 v[132:135], v10, s[44:47], s63 offen offset:1024 sc1\n"
    "buffer_load_dwordx4 v[136:139], v10, s[44:47], s63 offen offset:2048 sc1\n"
    "buffer_load_dwordx4 v[140:143], v10, s[44:47], s63 offen offset:3072 sc1\n"
    "s_waitcnt vmcnt(0)\n"
    "s_branch L_sweep_6_%=\n"
    "L_retry_9_%=:\n"
    "s_mov_b32 s71, 0\n"
    "s_mov_b32 s82, 2147483392\n"
    "L_loop_11_%=:\n"
    "buffer_load_dword v21, v20, s[44:47], s73 offen offset:908 sc1\n"
    "buffer_load_dwordx4 v[144:147], v1, s[44:47], s60 offen offset:0 sc1\n"
    "buffer_load_dwordx4 v[148:151], v1, s[44:47], s60 offen offset:1024 sc1\n"
    "buffer_load_dwordx4 v[152:155], v1, s[44:47], s60 offen offset:2048 sc1\n"
    "buffer_load_dwordx4 v[156:159], v1, s[44:47], s60 offen offset:3072 sc1\n"
    "buffer_load_dwordx4 v[160:163], v1, s[44:47], s61 offen offset:0 sc1\n"
    "buffer_load_dwordx4 v[164:167], v1, s[44:47], s61 offen offset:1024 sc1\n"
    "buffer_load_dwordx4 v[168:171], v1, s[44:47], s61 offen offset:2048 sc1\n"
    "buffer_load_dwordx4 v[172:175], v1, s[44:47], s61 offen offset:3072 sc1\n"
    "buffer_load_dwordx4 v[176:179], v1, s[44:47], s62 offen offset:0 sc1\n"
    "buffer_load_dwordx4 v[180:183], v1, s[44:47], s62 offen offset:1024 sc1\n"
    "s_add_u32 s71, s71, 1\n"
    "s_waitcnt vmcnt(10)\n"
    "v_xor_b32 v21, s58, v21\n"
    "v_cmp_lt_u32 vcc, 0xffff, v21\n"
    "s_cbranch_vccz L_go_10_%=\n"
    "s_cmp_lt_u32 s71, 4096\n"
    "s_cbranch_scc1 L_loop_11_%=\n"
    "s_branch L_go_10_%=\n"
    "L_redo_13_%=:\n"
    "s_waitcnt vmcnt(0) lgkmcnt(0)\n"
    "s_nop 15\n"
    "s_nop 15\n"
    "s_mov_b32 s82, 0\n"
    "L_rl_15_%=:\n"
    "buffer_load_dwordx4 v[144:147], v1, s[44:47], s60 offen offset:0 sc1\n"
    "buffer_load_dwordx4 v[148:151], v1, s[44:47], s60 offen offset:1024 sc1\n"
    "buffer_load_dwordx4 v[152:155], v1, s[44:47], s60 offen offset:2048 sc1\n"
    "buffer_load_dwordx4 v[156:159], v1, s[44:47], s60 offen offset:3072 sc1\n"
    "buffer_load_dwordx4 v[160:163], v1, s[44:47], s61 offen offset:0 sc1\n"
    "buffer_load_dwordx4 v[164:167], v1, s[44:47], s61 offen offset:1024 sc1\n"
    "buffer_load_dwordx4 v[168:171], v1, s[44:47], s61 offen offset:2048 sc1\n"
    "buffer_load_dwordx4 v[172:175], v1, s[44:47], s61 offen offset:3072 sc1\n"
    "buffer_load_dwordx4 v[176:179], v1, s[44:47], s62 offen offset:0 sc1\n"
    "buffer_load_dwordx4 v[180:183], v1, s[44:47], s62 offen offset:1024 sc1\n"
    "buffer_load_dwordx4 v[184:187], v1, s[44:47], s62 offen offset:2048 sc1\n"
    "buffer_load_dwordx4 v[188:191], v1, s[44:47], s62 offen offset:3072 sc1\n"
    "buffer_load_dwordx4 v[192:195], v1, s[44:47], s63 offen offset:0 sc1\n"
    "buffer_load_dwordx4 v[196:199], v1, s[44:47], s63 offen offset:1024 sc1\n"
    "buffer_load_dwordx4 v[200:203], v1, s[44:47], s63 offen offset:2048 sc1\n"
    "buffer_load_dwordx4 v[204:207], v1, s[44:47], s63 offen offset:3072 sc1\n"
    "s_waitcnt vmcnt(15)\n"
    "v_xor_b32 v147, s58, v147\n"
    "s_waitcnt vmcnt(14)\n"
    "v_xor_b32 v151, s58, v151\n"
    "s_waitcnt vmcnt(13)\n"
    "v_xor_b32 v155, s58, v155\n"
    "s_waitcnt vmcnt(12)\n"
    "v_xor_b32 v159, s58, v159\n"
    "s_waitcnt vmcnt(11)\n"
    "v_xor_b32 v163, s58, v163\n"
    "s_waitcnt vmcnt(10)\n"
    "v_xor_b32 v167, s58, v167\n"
    "s_waitcnt vmcnt(9)\n"
    "v_xor_b32 v171, s58, v171\n"
    "s_waitcnt vmcnt(8)\n"
    "v_xor_b32 v175, s58, v175\n"
    "s_waitcnt vmcnt(7)\n"
    "v_xor_b32 v179, s58, v179\n"
    "s_waitcnt vmcnt(6)\n"
    "v_xor_b32 v183, s58, v183\n"
    "s_waitcnt vmcnt(5)\n"
    "v_xor_b32 v187, s58, v187\n"
    "s_waitcnt vmcnt(4)\n"
    "v_xor_b32 v191, s58, v191\n"
    "s_waitcnt vmcnt(3)\n"
    "v_xor_b32 v195, s58, v195\n"
    "s_waitcnt vmcnt(2)\n"
    "v_xor_b32 v199, s58, v199\n"
    "s_waitcnt vmcnt(1)\n"
    "v_xor_b32 v203, s58, v203\n"
    "s_waitcnt vmcnt(0)\n"
    "v_xor_b32 v207, s58, v207\n"
    "s_add_u32 s82, s82, 1\n"
    "v_max_u32 v28, v147, v151\n"
    "v_max3_u32 v28, v28, v155, v159\n"
    "v_max3_u32 v28, v28, v163, v167\n"
    "v_max3_u32 v28, v28, v171, v175\n"
    "v_max3_u32 v28, v28, v179, v183\n"
    "v_max3_u32 v28, v28, v187, v191\n"
    "v_max3_u32 v28, v28, v195, v199\n"
    "v_max3_u32 v28, v28, v203, v207\n"
    "v_cmp_lt_u32 vcc, 0xffff, v28\n"
    "s_cbranch_vccz L_rd_16_%=\n"
    "s_cmp_lt_u32 s82, 4096\n"
    "s_cbranch_scc1 L_rl_15_%=\n"
    "L_rd_16_%=:\n"
    "v_accvgpr_read_b32 v80, a72\n"
    "v_accvgpr_read_b32 v81, a73\n"
    "v_accvgpr_read_b32 v82, a74\n"
    "v_accvgpr_read_b32 v83, a75\n"
    "v_accvgpr_read_b32 v84, a76\n"
    "v_accvgpr_read_b32 v85, a77\n"
    "v_accvgpr_read_b32 v86, a78\n"
    "v_accvgpr_read_b32 v87, a79\n"
    "v_permlane32_swap_b32 v80, v82\n"
    "v_permlane32_swap_b32 v81, v83\n"
    "v_permlane32_swap_b32 v84, v86\n"
    "v_permlane32_swap_b32 v85, v87\n"
    "v_lshlrev_b32 v32, 16, v80\n"
    "v_and_b32 v33, 0xffff0000, v80\n"
    "v_lshlrev_b32 v34, 16, v81\n"
    "v_and_b32 v35, 0xffff0000, v81\n"
    "v_lshlrev_b32 v36, 16, v82\n"
    "v_and_b32 v37, 0xffff0000, v82\n"
    "v_lshlrev_b32 v38, 16, v83\n"
    "v_and_b32 v39, 0xffff0000, v83\n"
    "v_lshlrev_b32 v40, 16, v84\n"
    "v_and_b32 v41, 0xffff0000, v84\n"
    "v_lshlrev_b32 v42, 16, v85\n"
    "v_and_b32 v43, 0xffff0000, v85\n"
    "v_lshlrev_b32 v44, 16, v86\n"
    "v_and_b32 v45, 0xffff0000, v86\n"
    "v_lshlrev_b32 v46, 16, v87\n"
    "v_and_b32 v47, 0xffff0000, v87\n"
    "v_mfma_f32_32x32x16_bf16 v[48:63], a[100:103], v[144:147], 0\n"
    "s_nop 0\n"
    "v_mfma_f32_32x32x16_bf16 v[32:47], a[96:99], v[144:147], v[32:47]\n"
    "v_mfma_f32_32x32x16_bf16 v[48:63], a[108:111], v[148:151], v[48:63]\n"
    "v_mfma_f32_32x32x16_bf16 v[32:47], a[104:107], v[148:151], v[32:47]\n"
    "v_mfma_f32_32x32x16_bf16 v[48:63], a[116:119], v[152:155], v[48:63]\n"
    "v_mfma_f32_32x32x16_bf16 v[32:47], a[112:115], v[152:155], v[32:47]\n"
    "v_mfma_f32_32x32x16_bf16 v[48:63], a[124:127], v[156:159], v[48:63]\n"
    "v_mfma_f32_32x32x16_bf16 v[32:47], a[120:123], v[156:159], v[32:47]\n"
    "v_mfma_f32_32x32x16_bf16 v[48:63], a[132:135], v[160:163], v[48:63]\n"
    "v_mfma_f32_32x32x16_bf16 v[32:47], a[128:131], v[160:163], v[32:47]\n"
    "v_mfma_f32_32x32x16_bf16 v[48:63], a[140:143], v[164:167], v[48:63]\n"
    "v_mfma_f32_32x32x16_bf16 v[32:47], a[136:139], v[164:167], v[32:47]\n"
    "v_mfma_f32_32x32x16_bf16 v[48:63], a[148:151], v[168:171], v[48:63]\n"
    "v_mfma_f32_32x32x16_bf16 v[32:47], a[144:147], v[168:171], v[32:47]\n"
    "v_mfma_f32_32x32x16_bf16 v[48:63], a[156:159], v[172:175], v[48:63]\n"
    "v_mfma_f32_32x32x16_bf16 v[32:47], a[152:155], v[172:175], v[32:47]\n"
    "v_mfma_f32_32x32x16_bf16 v[48:63], a[164:167], v[176:179], v[48:63]\n"
    "v_mfma_f32_32x32x16_bf16 v[32:47], a[160:163], v[176:179], v[32:47]\n"
    "v_mfma_f32_32x32x16_bf16 v[48:63], a[172:175], v[180:183], v[48:63]\n"
    "v_mfma_f32_32x32x16_bf16 v[32:47], a[168:171], v[180:183], v[32:47]\n"
    "v_mfma_f32_32x32x16_bf16 v[48:63], a[180:183], v[184:187], v[48:63]\n"
    "v_mfma_f32_32x32x16_bf16 v[32:47], a[176:179], v[184:187], v[32:47]\n"
    "v_mfma_f32_32x32x16_bf16 v[48:63], a[188:191], v[188:191], v[48:63]\n"
    "v_mfma_f32_32x32x16_bf16 v[32:47], a[184:187], v[188:191], v[32:47]\n"
    "v_mfma_f32_32x32x16_bf16 v[48:63], a[196:199], v[192:195], v[48:63]\n"
    "v_mfma_f32_32x32x16_bf16 v[32:47], a[192:195], v[192:195], v[32:47]\n"
    "v_mfma_f32_32x32x16_bf16 v[48:63], a[204:207], v[196:199], v[48:63]\n"
    "v_mfma_f32_32x32x16_bf16 v[48:63], a[212:215], v[200:203], v[48:63]\n"
    "v_mfma_f32_32x32x16_bf16 v[48:63], a[220:223], v[204:207], v[48:63]\n"
    "v_mfma_f32_32x32x16_bf16 v[32:47], a[200:203], v[196:199], v[32:47]\n"
    "v_mfma_f32_32x32x16_bf16 v[32:47], a[208:211], v[200:203], v[32:47]\n"
    "v_mfma_f32_32x32x16_bf16 v[32:47], a[216:219], v[204:207], v[32:47]\n"
    "s_nop 8\n"
    "ds_write_b128 v18, v[48:51] offset:0\n"
    "ds_write_b128 v18, v[52:55] offset:1024\n"
    "ds_write_b128 v18, v[56:59] offset:2048\n"
    "ds_write_b128 v18, v[60:63] offset:3072\n"
    "s_waitcnt vmcnt(0) lgkmcnt(0)\n"
    "s_nop 15\n"
    "s_nop 15\n"
    "s_branch L_go_14_%=\n"
    "L_retry_17_%=:\n"
    "s_mov_b32 s82, 0\n"
    "L_loop_19_%=:\n"
    "buffer_load_dword v31, v14, s[44:47], s73 offen sc1\n"
    "s_add_u32 s82, s82, 1\n"
    "s_waitcnt vmcnt(0)\n"
    "v_cmp_ne_u32 vcc, s56, v31\n"
    "s_cbranch_vccz L_done_20_%=\n"
    "s_cmp_lt_u32 s82, 65536\n"
    "s_cbranch_scc1 L_loop_19_%=\n"
    "L_done_20_%=:\n"
    "s_mov_b32 s82, 2147483392\n"
    "\n"
    "s_branch L_go_18_%=\n"
    "L_retry_21_%=:\n"
    "s_mov_b32 s82, 0\n"
    "L_loop_23_%=:\n"
    "buffer_load_dword v23, v14, s[44:47], s4 offen sc1\n"
    "s_add_u32 s82, s82, 1\n"
    "s_waitcnt vmcnt(0)\n"
    "v_cmp_ne_u32 vcc, s56, v23\n"
    "s_cbranch_vccz L_done_24_%=\n"
    "s_cmp_lt_u32 s82, 65536\n"
    "s_cbranch_scc1 L_loop_23_%=\n"
    "L_done_24_%=:\n"
    "s_mov_b32 s82, 2147483392\n"
    "buffer_load_dwordx4 v[248:251], v140, s[44:47], s90 offen offset:2048 sc1\n"
    "buffer_load_dwordx4 v[252:255], v140, s[44:47], s90 offen offset:3072 sc1\n"
    "buffer_load_dword v7, v1, s[44:47], s82 offen\n"
    "buffer_load_dword v7, v1, s[44:47], s82 offen\n"
    "buffer_load_dword v7, v1, s[44:47], s82 offen\n"
    "buffer_load_dword v7, v1, s[44:47], s82 offen\n"
    "buffer_load_dword v7, v1, s[44:47], s82 offen\n"
    "buffer_load_dword v7, v1, s[44:47], s82 offen\n"
    "s_branch L_go_22_%=\n"
    "L_retry_25_%=:\n"
    "s_mov_b32 s82, 0\n"
    "L_loop_27_%=:\n"
    "buffer_load_dword v28, v13, s[44:47], s65 offen offset:0 sc1\n"
    "s_add_u32 s82, s82, 1\n"
    "s_waitcnt vmcnt(0)\n"
    "v_cmp_ne_u32 vcc, s56, v28\n"
    "s_cbranch_vccz L_done_28_%=\n"
    "s_cmp_lt_u32 s82, 65536\n"
    "s_cbranch_scc1 L_loop_27_%=\n"
    "L_done_28_%=:\n"
    "s_mov_b32 s82, 2147483392\n"
    "buffer_load_dword v7, v1, s[44:47], s82 offen\n"
    "buffer_load_dword v7, v1, s[44:47], s82 offen\n"
    "buffer_load_dword v7, v1, s[44:47], s82 offen\n"
    "buffer_load_dword v7, v1, s[44:47], s82 offen\n"
    "buffer_load_dword v7, v1, s[44:47], s82 offen\n"
    "buffer_load_dword v7, v1, s[44:47], s82 offen\n"
    "buffer_load_dword v7, v1, s[44:47], s82 offen\n"
    "buffer_load_dword v7, v1, s[44:47], s82 offen\n"
    "s_branch L_go_26_%=\n"
    "L_retry_29_%=:\n"
    "s_mov_b32 s82, 0\n"
    "L_loop_31_%=:\n"
    "buffer_load_dword v29, v13, s[44:47], s65 offen offset:4 sc1\n"
    "s_add_u32 s82, s82, 1\n"
    "s_waitcnt vmcnt(0)\n"
    "v_cmp_ne_u32 vcc, s56, v29\n"
    "s_cbranch_vccz L_done_32_%=\n"
    "s_cmp_lt_u32 s82, 65536\n"
    "s_cbranch_scc1 L_loop_31_%=\n"
    "L_done_32_%=:\n"
    "s_mov_b32 s82, 2147483392\n"
    "buffer_load_dwordx4 v[188:191], v141, s[44:47], s89 offen offset:1024 sc1\n"
    "buffer_load_dwordx4 v[192:195], v141, s[44:47], s90 offen offset:0 sc1\n"
    "buffer_load_dword v7, v1, s[44:47], s82 offen\n"
    "buffer_load_dwordx4 v[196:199], v141, s[44:47], s90 offen offset:1024 sc1\n"
    "buffer_load_dword v7, v1, s[44:47], s82 offen\n"
    "buffer_load_dword v7, v1, s[44:47], s82 offen\n"
    "buffer_load_dword v7, v1, s[44:47], s82 offen\n"
    "buffer_load_dword v7, v1, s[44:47], s82 offen\n"
    "buffer_load_dword v7, v1, s[44:47], s82 offen\n"
    "buffer_load_dword v7, v1, s[44:47], s82 offen\n"
    "s_branch L_go_30_%=\n"
    "L_retry_33_%=:\n"
    "s_mov_b32 s71, 0\n"
    "s_mov_b32 s82, 2147483392\n"
    "L_loop_35_%=:\n"
    "buffer_load_dword v30, v12, s[44:47], s66 offen offset:896 sc1\n"
    "buffer_load_dwordx4 v[228:231], v141, s[44:47], s87 offen offset:3072 sc1\n"
    "buffer_load_dwordx4 v[232:235], v141, s[44:47], s88 offen offset:2048 sc1\n"
    "buffer_load_dwordx4 v[236:239], v141, s[44:47], s88 offen offset:3072 sc1\n"
    "buffer_load_dwordx4 v[240:243], v141, s[44:47], s89 offen offset:2048 sc1\n"
    "buffer_load_dwordx4 v[244:247], v141, s[44:47], s89 offen offset:3072 sc1\n"
    "buffer_load_dwordx4 v[248:251], v141, s[44:47], s90 offen offset:2048 sc1\n"
    "buffer_load_dwordx4 v[252:255], v141, s[44:47], s90 offen offset:3072 sc1\n"
    "s_add_u32 s71, s71, 1\n"
    "s_waitcnt vmcnt(7)\n"
    "v_cmp_eq_u32 vcc, -1, v30\n"
    "s_cbranch_vccz L_go_34_%=\n"
    "s_cmp_lt_u32 s71, 4096\n"
    "s_cbranch_scc1 L_loop_35_%=\n"
    "s_branch L_go_34_%=\n"
    "L_retry_37_%=:\n"
    "s_mov_b32 s83, 0\n"
    "s_mov_b32 s82, 2147483392\n"
    "L_loop_39_%=:\n"
    "buffer_load_dword v28, v12, s[44:47], s66 offen offset:2944 sc1\n"
    "buffer_load_dwordx4 v[200:203], v142, s[44:47], s84 offen offset:2048 sc1\n"
    "buffer_load_dwordx4 v[204:207], v142, s[44:47], s84 offen offset:3072 sc1\n"
    "buffer_load_dwordx4 v[208:211], v142, s[44:47], s85 offen offset:2048 sc1\n"
    "buffer_load_dwordx4 v[212:215], v142, s[44:47], s85 offen offset:3072 sc1\n"
    "buffer_load_dwordx4 v[216:219], v142, s[44:47], s86 offen offset:2048 sc1\n"
    "buffer_load_dwordx4 v[220:223], v142, s[44:47], s86 offen offset:3072 sc1\n"
    "buffer_load_dwordx4 v[224:227], v142, s[44:47], s87 offen offset:2048 sc1\n"
    "buffer_load_dwordx4 v[228:231], v142, s[44:47], s87 offen offset:3072 sc1\n"
    "buffer_load_dwordx4 v[232:235], v142, s[44:47], s88 offen offset:2048 sc1\n"
    "s_add_u32 s83, s83, 1\n"
    "s_waitcnt vmcnt(9)\n"
    "v_cmp_eq_u32 vcc, -1, v28\n"
    "s_cbranch_vccz L_go_38_%=\n"
    "s_cmp_lt_u32 s83, 4096\n"
    "s_cbranch_scc1 L_loop_39_%=\n"
    "s_branch L_go_38_%=\n"
    "L_redo_41_%=:\n"
    "s_waitcnt vmcnt(0) lgkmcnt(0)\n"
    "s_nop 15\n"
    "s_nop 15\n"
    "s_mov_b32 s82, 0\n"
    "L_rl_43_%=:\n"
    "buffer_load_dwordx4 v[144:147], v142, s[44:47], s84 offen offset:0 sc1\n"
    "buffer_load_dwordx4 v[148:151], v142, s[44:47], s84 offen offset:1024 sc1\n"
    "buffer_load_dwordx4 v[152:155], v142, s[44:47], s85 offen offset:0 sc1\n"
    "buffer_load_dwordx4 v[156:159], v142, s[44:47], s85 offen offset:1024 sc1\n"
    "buffer_load_dwordx4 v[160:163], v142, s[44:47], s86 offen offset:0 sc1\n"
    "buffer_load_dwordx4 v[164:167], v142, s[44:47], s86 offen offset:1024 sc1\n"
    "buffer_load_dwordx4 v[168:171], v142, s[44:47], s87 offen offset:0 sc1\n"
    "buffer_load_dwordx4 v[172:175], v142, s[44:47], s87 offen offset:1024 sc1\n"
    "buffer_load_dwordx4 v[176:179], v142, s[44:47], s88 offen offset:0 sc1\n"
    "buffer_load_dwordx4 v[180:183], v142, s[44:47], s88 offen offset:1024 sc1\n"
    "buffer_load_dwordx4 v[184:187], v142, s[44:47], s89 offen offset:0 sc1\n"
    "buffer_load_dwordx4 v[188:191], v142, s[44:47], s89 offen offset:1024 sc1\n"
    "buffer_load_dwordx4 v[192:195], v142, s[44:47], s90 offen offset:0 sc1\n"
    "buffer_load_dwordx4 v[196:199], v142, s[44:47], s90 offen offset:1024 sc1\n"
    "buffer_load_dwordx4 v[200:203], v142, s[44:47], s84 offen offset:2048 sc1\n"
    "buffer_load_dwordx4 v[204:207], v142, s[44:47], s84 offen offset:3072 sc1\n"
    "buffer_load_dwordx4 v[208:211], v142, s[44:47], s85 offen offset:2048 sc1\n"
    "buffer_load_dwordx4 v[212:215], v142, s[44:47], s85 offen offset:3072 sc1\n"
    "buffer_load_dwordx4 v[216:219], v142, s[44:47], s86 offen offset:2048 sc1\n"
    "buffer_load_dwordx4 v[220:223], v142, s[44:47], s86 offen offset:3072 sc1\n"
    "buffer_load_dwordx4 v[224:227], v142, s[44:47], s87 offen offset:2048 sc1\n"
    "buffer_load_dwordx4 v[228:231], v142, s[44:47], s87 offen offset:3072 sc1\n"
    "buffer_load_dwordx4 v[232:235], v142, s[44:47], s88 offen offset:2048 sc1\n"
    "buffer_load_dwordx4 v[236:239], v142, s[44:47], s88 offen offset:3072 sc1\n"
    "buffer_load_dwordx4 v[240:243], v142, s[44:47], s89 offen offset:2048 sc1\n"
    "buffer_load_dwordx4 v[244:247], v142, s[44:47], s89 offen offset:3072 sc1\n"
    "buffer_load_dwordx4 v[248:251], v142, s[44:47], s90 offen offset:2048 sc1\n"
    "buffer_load_dwordx4 v[252:255], v142, s[44:47], s90 offen offset:3072 sc1\n"
    "s_add_u32 s82, s82, 1\n"
    "s_waitcnt vmcnt(27)\n"
    "v_max_u32 v30, v144, v145\n"
    "v_max3_u32 v30, v30, v146, v147\n"
    "s_waitcnt vmcnt(26)\n"
    "v_max3_u32 v30, v30, v148, v149\n"
    "v_max3_u32 v30, v30, v150, v151\n"
    "s_waitcnt vmcnt(25)\n"
    "v_max3_u32 v30, v30, v152, v153\n"
    "v_max3_u32 v30, v30, v154, v155\n"
    "s_waitcnt vmcnt(24)\n"
    "v_max3_u32 v30, v30, v156, v157\n"
    "v_max3_u32 v30, v30, v158, v159\n"
    "s_waitcnt vmcnt(23)\n"
    "v_max3_u32 v30, v30, v160, v161\n"
    "v_max3_u32 v30, v30, v162, v163\n"
    "s_waitcnt vmcnt(22)\n"
    "v_max3_u32 v30, v30, v164, v165\n"
    "v_max3_u32 v30, v30, v166, v167\n"
    "s_waitcnt vmcnt(21)\n"
    "v_max3_u32 v30, v30, v168, v169\n"
    "v_max3_u32 v30, v30, v170, v171\n"
    "s_waitcnt vmcnt(20)\n"
    "v_max3_u32 v30, v30, v172, v173\n"
    "v_max3_u32 v30, v30, v174, v175\n"
    "s_waitcnt vmcnt(19)\n"
    "v_max3_u32 v30, v30, v176, v177\n"
    "v_max3_u32 v30, v30, v178, v179\n"
    "s_waitcnt vmcnt(18)\n"
    "v_max3_u32 v30, v30, v180, v181\n"
    "v_max3_u32 v30, v30, v182, v183\n"
    "s_waitcnt vmcnt(17)\n"
    "v_max3_u32 v30, v30, v184, v185\n"
    "v_max3_u32 v30, v30, v186, v187\n"
    "s_waitcnt vmcnt(16)\n"
    "v_max3_u32 v30, v30, v188, v189\n"
    "v_max3_u32 v30, v30, v190, v191\n"
    "s_waitcnt vmcnt(15)\n"
    "v_max3_u32 v30, v30, v192, v193\n"
    "v_max3_u32 v30, v30, v194, v195\n"
    "s_waitcnt vmcnt(14)\n"
    "v_max3_u32 v30, v30, v196, v197\n"
    "v_max3_u32 v30, v30, v198, v199\n"
    "s_waitcnt vmcnt(13)\n"
    "v_max3_u32 v30, v30, v200, v201\n"
    "v_max3_u32 v30, v30, v202, v203\n"
    "s_waitcnt vmcnt(12)\n"
    "v_max3_u32 v30, v30, v204, v205\n"
    "v_max3_u32 v30, v30, v206, v207\n"
    "s_waitcnt vmcnt(11)\n"
    "v_max3_u32 v30, v30, v208, v209\n"
    "v_max3_u32 v30, v30, v210, v211\n"
    "s_waitcnt vmcnt(10)\n"
    "v_max3_u32 v30, v30, v212, v213\n"
    "v_max3_u32 v30, v30, v214, v215\n"
    "s_waitcnt vmcnt(9)\n"
    "v_max3_u32 v30, v30, v216, v217\n"
    "v_max3_u32 v30, v30, v218, v219\n"
    "s_waitcnt vmcnt(8)\n"
    "v_max3_u32 v30, v30, v220, v221\n"
    "v_max3_u32 v30, v30, v222, v223\n"
    "s_waitcnt vmcnt(7)\n"
    "v_max3_u32 v30, v30, v224, v225\n"
    "v_max3_u32 v30, v30, v226, v227\n"
    "s_waitcnt vmcnt(6)\n"
    "v_max3_u32 v30, v30, v228, v229\n"
    "v_max3_u32 v30, v30, v230, v231\n"
    "s_waitcnt vmcnt(5)\n"
    "v_max3_u32 v30, v30, v232, v233\n"
    "v_max3_u32 v30, v30, v234, v235\n"
    "s_waitcnt vmcnt(4)\n"
    "v_max3_u32 v30, v30, v236, v237\n"
    "v_max3_u32 v30, v30, v238, v239\n"
    "s_waitcnt vmcnt(3)\n"
    "v_max3_u32 v30, v30, v240, v241\n"
    "v_max3_u32 v30, v30, v242, v243\n"
    "s_waitcnt vmcnt(2)\n"
    "v_max3_u32 v30, v30, v244, v245\n"
    "v_max3_u32 v30, v30, v246, v247\n"
    "s_waitcnt vmcnt(1)\n"
    "v_max3_u32 v30, v30, v248, v249\n"
    "v_max3_u32 v30, v30, v250, v251\n"
    "s_waitcnt vmcnt(0)\n"
    "v_max3_u32 v30, v30, v252, v253\n"
    "v_max3_u32 v30, v30, v254, v255\n"
    "v_cmp_eq_u32 vcc, -1, v30\n"
    "s_cbranch_vccz L_rd_44_%=\n"
    "s_cmp_lt_u32 s82, 4096\n"
    "s_cbranch_scc1 L_rl_43_%=\n"
    "L_rd_44_%=:\n"
    "v_mfma_f32_32x32x16_bf16 v[64:79], a[0:3], v[144:147], 0\n"
    "v_mfma_f32_32x32x16_bf16 v[80:95], a[4:7], v[144:147], 0\n"
    "v_mfma_f32_32x32x16_bf16 v[64:79], a[8:11], v[148:151], v[64:79]\n"
    "v_mfma_f32_32x32x16_bf16 v[80:95], a[12:15], v[148:151], v[80:95]\n"
    "v_mfma_f32_32x32x16_bf16 v[64:79], a[16:19], v[152:155], v[64:79]\n"
    "v_mfma_f32_32x32x16_bf16 v[80:95], a[20:23], v[152:155], v[80:95]\n"
    "v_mfma_f32_32x32x16_bf16 v[64:79], a[24:27], v[156:159], v[64:79]\n"
    "v_mfma_f32_32x32x16_bf16 v[80:95], a[28:31], v[156:159], v[80:95]\n"
    "v_mfma_f32_32x32x16_bf16 v[64:79], a[32:35], v[160:163], v[64:79]\n"
    "v_mfma_f32_32x32x16_bf16 v[80:95], a[36:39], v[160:163], v[80:95]\n"
    "v_mfma_f32_32x32x16_bf16 v[64:79], a[40:43], v[164:167], v[64:79]\n"
    "v_mfma_f32_32x32x16_bf16 v[80:95], a[44:47], v[164:167], v[80:95]\n"
    "v_mfma_f32_32x32x16_bf16 v[64:79], a[48:51], v[168:171], v[64:79]\n"
    "v_mfma_f32_32x32x16_bf16 v[80:95], a[52:55], v[168:171], v[80:95]\n"
    "v_mfma_f32_32x32x16_bf16 v[64:79], a[56:59], v[172:175], v[64:79]\n"
    "v_mfma_f32_32x32x16_bf16 v[80:95], a[60:63], v[172:175], v[80:95]\n"
    "v_mfma_f32_32x32x16_bf16 v[64:79], a[64:67], v[176:179], v[64:79]\n"
    "v_mfma_f32_32x32x16_bf16 v[80:95], a[68:71], v[176:179], v[80:95]\n"
    "v_mfma_f32_32x32x16_bf16 v[64:79], a[72:75], v[180:183], v[64:79]\n"
    "v_mfma_f32_32x32x16_bf16 v[80:95], a[76:79], v[180:183], v[80:95]\n"
    "v_mfma_f32_32x32x16_bf16 v[64:79], a[80:83], v[184:187], v[64:79]\n"
    "v_mfma_f32_32x32x16_bf16 v[80:95], a[84:87], v[184:187], v[80:95]\n"
    "v_mfma_f32_32x32x16_bf16 v[64:79], a[88:91], v[188:191], v[64:79]\n"
    "v_mfma_f32_32x32x16_bf16 v[80:95], a[92:95], v[188:191], v[80:95]\n"
    "v_mfma_f32_32x32x16_bf16 v[64:79], a[96:99], v[192:195], v[64:79]\n"
    "v_mfma_f32_32x32x16_bf16 v[80:95], a[100:103], v[192:195], v[80:95]\n"
    "v_mfma_f32_32x32x16_bf16 v[64:79], a[104:107], v[196:199], v[64:79]\n"
    "v_mfma_f32_32x32x16_bf16 v[80:95], a[108:111], v[196:199], v[80:95]\n"
    "v_mfma_f32_32x32x16_bf16 v[64:79], a[112:115], v[200:203], v[64:79]\n"
    "v_mfma_f32_32x32x16_bf16 v[80:95], a[116:119], v[200:203], v[80:95]\n"
    "v_mfma_f32_32x32x16_bf16 v[64:79], a[120:123], v[204:207], v[64:79]\n"
    "v_mfma_f32_32x32x16_bf16 v[80:95], a[124:127], v[204:207], v[80:95]\n"
    "v_mfma_f32_32x32x16_bf16 v[64:79], a[128:131], v[208:211], v[64:79]\n"
    "v_mfma_f32_32x32x16_bf16 v[80:95], a[132:135], v[208:211], v[80:95]\n"
    "v_mfma_f32_32x32x16_bf16 v[64:79], a[136:139], v[212:215], v[64:79]\n"
    "v_mfma_f32_32x32x16_bf16 v[80:95], a[140:143], v[212:215], v[80:95]\n"
    "v_mfma_f32_32x32x16_bf16 v[64:79], a[144:147], v[216:219], v[64:79]\n"
    "v_mfma_f32_32x32x16_bf16 v[80:95], a[148:151], v[216:219], v[80:95]\n"
    "v_mfma_f32_32x32x16_bf16 v[64:79], a[152:155], v[220:223], v[64:79]\n"
    "v_mfma_f32_32x32x16_bf16 v[80:95], a[156:159], v[220:223], v[80:95]\n"
    "v_mfma_f32_32x32x16_bf16 v[64:79], a[160:163], v[224:227], v[64:79]\n"
    "v_mfma_f32_32x32x16_bf16 v[80:95], a[164:167], v[224:227], v[80:95]\n"
    "v_mfma_f32_32x32x16_bf16 v[64:79], a[168:171], v[228:231], v[64:79]\n"
    "v_mfma_f32_32x32x16_bf16 v[80:95], a[172:175], v[228:231], v[80:95]\n"
    "v_mfma_f32_32x32x16_bf16 v[64:79], a[176:179], v[232:235], v[64:79]\n"
    "v_mfma_f32_32x32x16_bf16 v[80:95], a[180:183], v[232:235], v[80:95]\n"
    "v_mfma_f32_32x32x16_bf16 v[80:95], a[188:191], v[236:239], v[80:95]\n"
    "v_mfma_f32_32x32x16_bf16 v[80:95], a[196:199], v[240:243], v[80:95]\n"
    "v_mfma_f32_32x32x16_bf16 v[80:95], a[204:207], v[244:247], v[80:95]\n"
    "v_mfma_f32_32x32x16_bf16 v[80:95], a[212:215], v[248:251], v[80:95]\n"
    "v_mfma_f32_32x32x16_bf16 v[80:95], a[220:223], v[252:255], v[80:95]\n"
    "v_mfma_f32_32x32x16_bf16 v[64:79], a[184:187], v[236:239], v[64:79]\n"
    "v_mfma_f32_32x32x16_bf16 v[64:79], a[192:195], v[240:243], v[64:79]\n"
    "v_mfma_f32_32x32x16_bf16 v[64:79], a[200:203], v[244:247], v[64:79]\n"
    "v_mfma_f32_32x32x16_bf16 v[64:79], a[208:211], v[248:251], v[64:79]\n"
    "v_mfma_f32_32x32x16_bf16 v[64:79], a[216:219], v[252:255], v[64:79]\n"
    "s_nop 11\n"
    "ds_write_b128 v8, v[72:75] offset:0\n"
    "ds_write_b128 v8, v[76:79] offset:1024\n"
    "ds_write_b128 v8, v[80:83] offset:2048\n"
    "ds_write_b128 v8, v[84:87] offset:3072\n"
    "ds_write_b128 v8, v[88:91] offset:4096\n"
    "ds_write_b128 v8, v[92:95] offset:5120\n"
    "s_waitcnt vmcnt(0) lgkmcnt(0)\n"
    "s_nop 15\n"
    "s_nop 15\n"
    "s_branch L_go_42_%=\n"
    :: "s"(__builtin_amdgcn_kernarg_segment_ptr()), "s"(blockIdx.x), "v"(tid), "s"((unsigned)(size_t)lds)
    : "s4", "s5", "s6", "s7", "s8", "s9", "s10", "s11", "s12", "s13", "s14", "s15", "s16", "s17", "s18", "s19", "s20", "s21", "s22", "s23", "s24", "s25", "s26", "s27", "s28", "s29", "s30", "s31", "s33", "s34", "s35", "s36", "s37", "s38", "s39", "s40", "s41", "s42", "s43", "s44", "s45", "s46", "s47", "s48", "s49", "s50", "s51", "s52", "s53", "s54", "s55", "s56", "s57", "s58", "s59", "s60", "s61", "s62", "s63", "s64", "s65", "s66", "s67", "s68", "s69", "s70", "s71", "s72", "s73", "s74", "s75", "s76", "s77", "s78", "s79", "s80", "s81", "s82", "s83", "s84", "s85", "s86", "s87", "s88", "s89", "s90", "s91", "s92", "s93", "s94", "s95", "s96", "s97", "s98", "s99", "v1", "v2", "v3", "v4", "v5", "v6", "v7", "v8", "v9", "v10", "v11", "v12", "v13", "v14", "v15", "v16", "v17", "v18", "v19", "v20", "v21", "v22", "v23", "v24", "v25", "v26", "v27", "v28", "v29", "v30", "v31", "v32", "v33", "v34", "v35", "v36", "v37", "v38", "v39", "v40", "v41", "v42", "v43", "v44", "v45", "v46", "v47", "v48", "v49", "v50", "v51", "v52", "v53", "v54", "v55", "v56", "v57", "v58", "v59", "v60", "v61", "v62", "v63", "v64", "v65", "v66", "v67", "v68", "v69", "v70", "v71", "v72", "v73", "v74", "v75", "v76", "v77", "v78", "v79", "v80", "v81", "v82", "v83", "v84", "v85", "v86", "v87", "v88", "v89", "v90", "v91", "v92", "v93", "v94", "v95", "v96", "v97", "v98", "v99", "v100", "v101", "v102", "v103", "v104", "v105", "v106", "v107", "v108", "v109", "v110", "v111", "v112", "v113", "v114", "v115", "v116", "v117", "v118", "v119", "v120", "v121", "v122", "v123", "v124", "v125", "v126", "v127", "v128", "v129", "v130", "v131", "v132", "v133", "v134", "v135", "v136", "v137", "v138", "v139", "v140", "v141", "v142", "v143", "v144", "v145", "v146", "v147", "v148", "v149", "v150", "v151", "v152", "v153", "v154", "v155", "v156", "v157", "v158", "v159", "v160", "v161", "v162", "v163", "v164", "v165", "v166", "v167", "v168", "v169", "v170", "v171", "v172", "v173", "v174", "v175", "v176", "v177", "v178", "v179", "v180", "v181", "v182", "v183", "v184", "v185", "v186", "v187", "v188", "v189", "v190", "v191", "v192", "v193", "v194", "v195", "v196", "v197", "v198", "v199", "v200", "v201", "v202", "v203", "v204", "v205", "v206", "v207", "v208", "v209", "v210", "v211", "v212", "v213", "v214", "v215", "v216", "v217", "v218", "v219", "v220", "v221", "v222", "v223", "v224", "v225", "v226", "v227", "v228", "v229", "v230", "v231", "v232", "v233", "v234", "v235", "v236", "v237", "v238", "v239", "v240", "v241", "v242", "v243", "v244", "v245", "v246", "v247", "v248", "v249", "v250", "v251", "v252", "v253", "v254", "v255", "a0", "a1", "a2", "a3", "a4", "a5", "a6", "a7", "a8", "a9", "a10", "a11", "a12", "a13", "a14", "a15", "a16", "a17", "a18", "a19", "a20", "a21", "a22", "a23", "a24", "a25", "a26", "a27", "a28", "a29", "a30", "a31", "a32", "a33", "a34", "a35", "a36", "a37", "a38", "a39", "a40", "a41", "a42", "a43", "a44", "a45", "a46", "a47", "a48", "a49", "a50", "a51", "a52", "a53", "a54", "a55", "a56", "a57", "a58", "a59", "a60", "a61", "a62", "a63", "a64", "a65", "a66", "a67", "a68", "a69", "a70", "a71", "a72", "a73", "a74", "a75", "a76", "a77", "a78", "a79", "a80", "a81", "a82", "a83", "a84", "a85", "a86", "a87", "a88", "a89", "a90", "a91", "a92", "a93", "a94", "a95", "a96", "a97", "a98", "a99", "a100", "a101", "a102", "a103", "a104", "a105", "a106", "a107", "a108", "a109", "a110", "a111", "a112", "a113", "a114", "a115", "a116", "a117", "a118", "a119", "a120", "a121", "a122", "a123", "a124", "a125", "a126", "a127", "a128", "a129", "a130", "a131", "a132", "a133", "a134", "a135", "a136", "a137", "a138", "a139", "a140", "a141", "a142", "a143", "a144", "a145", "a146", "a147", "a148", "a149", "a150", "a151", "a152", "a153", "a154", "a155", "a156", "a157", "a158", "a159", "a160", "a161", "a162", "a163", "a164", "a165", "a166", "a167", "a168", "a169", "a170", "a171", "a172", "a173", "a174", "a175", "a176", "a177", "a178", "a179", "a180", "a181", "a182", "a183", "a184", "a185", "a186", "a187", "a188", "a189", "a190", "a191", "a192", "a193", "a194", "a195", "a196", "a197", "a198", "a199", "a200", "a201", "a202", "a203", "a204", "a205", "a206", "a207", "a208", "a209", "a210", "a211", "a212", "a213", "a214", "a215", "a216", "a217", "a218", "a219", "a220", "a221", "a222", "a223", "a224", "a225", "a226", "a227", "a228", "a229", "a230", "a231", "a232", "a233", "a234", "a235", "a236", "a237", "a238", "a239", "a240", "a241", "a242", "a243", "a244", "a245", "a246", "a247", "a248", "a249", "a250", "a251", "a252", "a253", "a254", "a255", "vcc", "scc", "memory");
}


extern "C" void kernel_launch(void* const* d_in, const int* in_sizes, int n_in, void* d_out, int out_size, void* d_ws, size_t ws_size,
                              hipStream_t stream) {

  fused_block<<<256, 256, 0, stream>>>(d_in[0], d_in[1], d_in[2], d_in[4], d_in[5], d_in[6], d_out, d_ws);

}
```
